```python
import torch

def _softmax(x, axis=-1):
    return torch.softmax(x, dim=axis)
def _gelu(x):
    return torch.nn.functional.gelu(x, approximate="tanh")
def _silu(x):
    return torch.nn.functional.silu(x)
def _sigmoid(x):
    return torch.sigmoid(x)
def _layernorm(x, axis=-1, eps=1e-5):
    m = x.mean(dim=axis, keepdim=True)
    v = x.var(dim=axis, keepdim=True, unbiased=False)
    return (x - m) / torch.sqrt(v + eps)
def _scatter_add(tgt, idx, src, axis=0):
    return tgt.clone().index_add_(axis, idx, src)
def _segment_sum(src, seg, n):
    out = torch.zeros((n,) + src.shape[1:], dtype=src.dtype)
    return out.index_add_(0, seg, src)
def _pad(x, pw):
    flat = tuple(v for pair in reversed(pw) for v in pair)
    return torch.nn.functional.pad(x, flat)

kernel_name = 'gated_mix__sliding_block_cached__glu_gated_swiglu__decode'

def setup_inputs():

    _w = (lambda a: (lambda t: t.to(torch.bfloat16) if t.is_floating_point() else t)(torch.as_tensor(a)))
    def _A_setup():
        B, Sq, D = 8, 1, 512
        Hq, Hkv, Sk, Dh = 32, 32, 4096, 128
        assert Hq % Hkv == 0, (Hq, Hkv)
        return {
            "x": _w((lambda *s: torch.randn(*s, dtype=torch.bfloat16) * 0.05)(B, Sq, D)),
            "k_cache": _w((lambda *s: torch.randn(*s, dtype=torch.bfloat16) * 3.2)(B, Hkv, Sk, Dh)),
            "v_cache": _w((lambda *s: torch.randn(*s, dtype=torch.bfloat16) * 0.05)(B, Hkv, Sk, Dh)),
            "wq": _w((lambda *s: torch.randn(*s, dtype=torch.bfloat16) * 3.2)(D, Hq * Dh) * (D ** -0.5)),
            "wo": _w((lambda *s: torch.randn(*s, dtype=torch.bfloat16) * 0.05)(Hq * Dh, D) * ((Hq * Dh) ** -0.5)),
        }
    _A_in = _A_setup()

    def _B_setup():
        B, S, D, F = 8, 1, 512, 1376
        return {
            "x": _w((lambda *s: torch.randn(*s, dtype=torch.bfloat16) * 0.05)(B, S, D)),
            "w1": _w((lambda *s: torch.randn(*s, dtype=torch.bfloat16) * 0.2)(D, F) * (2.0 / D) ** 0.5),
            "w3": _w((lambda *s: torch.randn(*s, dtype=torch.bfloat16) * 0.2)(D, F) * (2.0 / D) ** 0.5),
            "w2": _w((lambda *s: torch.randn(*s, dtype=torch.bfloat16) * 0.2)(F, D) * (2.0 / F) ** 0.5),
        }
    _B_in = _B_setup()

    return {
        "A_x": _A_in["x"],
        "A_k_cache": _A_in["k_cache"],
        "A_v_cache": _A_in["v_cache"],
        "A_wq": _A_in["wq"],
        "A_wo": _A_in["wo"],
        "B_x": _B_in["x"],
        "B_w1": _B_in["w1"],
        "B_w3": _B_in["w3"],
        "B_w2": _B_in["w2"],
        "gate_w": _w((lambda *s: torch.randn(*s, dtype=torch.bfloat16) * 0.05)(512) * 16.0),
    }

def reference(A_x, A_k_cache, A_v_cache, A_wq, A_wo, B_x, B_w1, B_w3, B_w2, gate_w):
    def _A_ref(x, k_cache, v_cache, wq, wo):
        B, Sq, D = x.shape
        Hq, Dh = 32, 128
        Hkv, Sk = k_cache.shape[1], k_cache.shape[2]
        W = min(4096, max(256, Sk // 4))
        rep = Hq // Hkv
        q = (lambda x, *d: x.permute(*d))((x @ wq).reshape(B, Sq, Hq, Dh), 0, 2, 1, 3)
        k = (lambda x, n, axis: torch.repeat_interleave(x, n, dim=axis))(k_cache[:, :, Sk - W:, :], rep, axis=1)
        v = (lambda x, n, axis: torch.repeat_interleave(x, n, dim=axis))(v_cache[:, :, Sk - W:, :], rep, axis=1)
        scores = torch.einsum("bhqd,bhkd->bhqk", q, k) * (Dh ** -0.5)
        p = _softmax(scores, axis=-1)
        o = torch.einsum("bhqk,bhkd->bhqd", p, v)
        return (lambda x, *d: x.permute(*d))(o, 0, 2, 1, 3).reshape(B, Sq, Hq * Dh) @ wo

    def _B_ref(x, w1, w3, w2):
        gate = torch.einsum("bsd,df->bsf", x, w1)
        up = torch.einsum("bsd,df->bsf", x, w3)
        h = _silu(gate) * up
        out = torch.einsum("bsf,fd->bsd", h, w2)
        return out

    x = A_x
    g = _sigmoid((x * gate_w).sum(-1, keepdims=True))
    a = _A_ref(x, A_k_cache, A_v_cache, A_wq, A_wo)
    b = _B_ref(x, B_w1, B_w3, B_w2)
    return g * a + (1 - g) * b
```

```cpp
#include <hip/hip_runtime.h>
#include <hip/hip_bf16.h>

#define KN_R 4
#define KN_N0 20
#define KN_RW 2

namespace {

typedef unsigned v4u __attribute__((ext_vector_type(4)));
typedef unsigned v2u __attribute__((ext_vector_type(2)));
typedef float v4f __attribute__((ext_vector_type(4)));
typedef float v2f __attribute__((ext_vector_type(2)));
typedef short v4s __attribute__((ext_vector_type(4)));
typedef __bf16 v8bf __attribute__((ext_vector_type(8)));
typedef __bf16 v2bf __attribute__((ext_vector_type(2)));
#define LAS __attribute__((address_space(3)))
#define GAS __attribute__((address_space(1)))
typedef __amdgpu_buffer_rsrc_t rsrc_t;

constexpr int DM = 512, NH = 32, SK = 4096, WIN = 1024, FF = 1376;
constexpr int NCOL = 4096 + 2 * FF;
constexpr int WS_EP = 0, WS_PX = 4096, WS_FX = 512 * 1024, WS_BYTES = WS_FX + 8 * 32 * 32 * 16 * 8;
constexpr int AUX_NT = 2, AUX_SC1 = 16, OOB = 0x7ffffff0;
constexpr unsigned TAG_STEP = 0x9E3779B9u;
constexpr unsigned SPIN_CAP = 1u << 16;
constexpr int L_STG = 0;
constexpr int L_RED = 32768;
constexpr int L_OP = L_RED + 8192;
constexpr int L_Q = L_OP + 4096;
constexpr int L_H = L_Q + 256;
constexpr int L_ST = L_H + 128;
constexpr int L_G = L_ST + 64;
constexpr int L_FL = L_G + 16;
constexpr int L_BYTES = L_FL + 16;

__device__ __forceinline__ rsrc_t mk_rsrc(const void* p, int bytes) { return __builtin_amdgcn_make_buffer_rsrc((void*)p, 0, bytes, 0x00020000); }
__device__ __forceinline__ unsigned pk_bf16(float lo, float hi) { v2bf r = {(__bf16)lo, (__bf16)hi}; return __builtin_bit_cast(unsigned, r); }
__device__ __forceinline__ float rbf(float x) { return (float)(__bf16)x; }
__device__ __forceinline__ float bf_lo(unsigned u) { return __builtin_bit_cast(float, u << 16); }
__device__ __forceinline__ float bf_hi(unsigned u) { return __builtin_bit_cast(float, u & 0xffff0000u); }
__device__ __forceinline__ float dot2(unsigned a, unsigned b, float c) { return __builtin_amdgcn_fdot2_f32_bf16(__builtin_bit_cast(v2bf, a), __builtin_bit_cast(v2bf, b), c, false); }
__device__ __forceinline__ v4f mfma(v4u a, v4u b, v4f c) { return __builtin_amdgcn_mfma_f32_16x16x32_bf16(__builtin_bit_cast(v8bf, a), __builtin_bit_cast(v8bf, b), c, 0, 0, 0); }
template <int CTRL> __device__ __forceinline__ unsigned dppu(unsigned x) { return (unsigned)__builtin_amdgcn_mov_dpp((int)x, CTRL, 0xf, 0xf, true); }
template <int CTRL> __device__ __forceinline__ float dppf(float x) { return __builtin_bit_cast(float, dppu<CTRL>(__builtin_bit_cast(unsigned, x))); }
constexpr int XOR1 = 0xB1, XOR2 = 0x4E, XOR7 = 0x141, XOR8 = 0x128, BCAST = 0x150;
__device__ __forceinline__ float rows_sum(float x) {
  auto s = __builtin_amdgcn_permlane16_swap(__float_as_uint(x), __float_as_uint(x), false, false);
  x = __uint_as_float(s[0]) + __uint_as_float(s[1]);
  auto t = __builtin_amdgcn_permlane32_swap(__float_as_uint(x), __float_as_uint(x), false, false);
  return __uint_as_float(t[0]) + __uint_as_float(t[1]);
}
__device__ __forceinline__ float rows_max(float x) {
  auto s = __builtin_amdgcn_permlane16_swap(__float_as_uint(x), __float_as_uint(x), false, false);
  x = fmaxf(__uint_as_float(s[0]), __uint_as_float(s[1]));
  auto t = __builtin_amdgcn_permlane32_swap(__float_as_uint(x), __float_as_uint(x), false, false);
  return fmaxf(__uint_as_float(t[0]), __uint_as_float(t[1]));
}
__device__ __forceinline__ float wave_sum(float x) { x += dppf<XOR8>(x); x += dppf<XOR7>(x); x += dppf<XOR2>(x); x += dppf<XOR1>(x); return rows_sum(x); }
__device__ __forceinline__ float wave_max(float x) { x = fmaxf(x, dppf<XOR8>(x)); x = fmaxf(x, dppf<XOR7>(x)); x = fmaxf(x, dppf<XOR2>(x)); x = fmaxf(x, dppf<XOR1>(x)); return rows_max(x); }
template <int N> __device__ __forceinline__ void wait_vm() { asm volatile("s_waitcnt vmcnt(%0)" ::"i"(N) : "memory"); }
__device__ __forceinline__ void wait_lds() { asm volatile("s_waitcnt lgkmcnt(0)" ::: "memory"); }
__device__ __forceinline__ void barrier() { asm volatile("s_waitcnt lgkmcnt(0)\n\ts_barrier" ::: "memory"); }
__device__ __forceinline__ void lds_wait_ge(LAS unsigned* p, unsigned want) {
  while ((unsigned)__builtin_amdgcn_readfirstlane(__hip_atomic_load(p, __ATOMIC_RELAXED, __HIP_MEMORY_SCOPE_WORKGROUP)) < want) __builtin_amdgcn_s_sleep(2);
  asm volatile("" ::: "memory");
}
__device__ __forceinline__ void lds_signal(LAS unsigned* p, int lane) {
  wait_lds();
  if (lane == 0) __hip_atomic_fetch_add(p, 1u, __ATOMIC_RELAXED, __HIP_MEMORY_SCOPE_WORKGROUP);
  asm volatile("" ::: "memory");
}
#define SB() __builtin_amdgcn_sched_barrier(0)
constexpr int rev4(int i) { return ((i & 1) << 3) | ((i & 2) << 1) | ((i & 4) >> 1) | ((i & 8) >> 3); }
__device__ __forceinline__ int wg_of(int b, int h) { return (h >> 2) + 8 * ((h & 3) + 4 * b); }

#define STAMP(i) do {} while (0)

struct Stream {
  rsrc_t rk, rv;
  int voff, lane;
  LAS unsigned char* L;
  v4u a[64];
  v4u q;
  float lv[5];
  unsigned pp[2], msk[4];
  float s[2];
  v4f acc;
  int w;
};
constexpr int k_done(int n) { int d = n < KN_N0 ? 0 : 2 * (n - KN_N0 + 1); int lim = n - KN_R + 1; d = d < lim ? d : lim; return d < 0 ? 0 : (d > 32 ? 32 : d); }
constexpr int soft_step() { int n = 0; while (k_done(n) < 32) ++n; return n + 1; }
constexpr int v_done(int n) { int d = n <= soft_step() ? 0 : 2 * (n - soft_step()); int lim = n - KN_R + 1 - 32; d = d < lim ? d : lim; return d < 0 ? 0 : (d > 32 ? 32 : d); }

template <int I> __device__ __forceinline__ void eat_k(Stream& S) {
  float d = dot2(S.a[I].x, S.q.x, 0.f);
  d = dot2(S.a[I].y, S.q.y, d); d = dot2(S.a[I].z, S.q.z, d); d = dot2(S.a[I].w, S.q.w, d);
  const int c = S.lane;
#define TR_LEVEL(LV, CTRL, BIT) \
  if constexpr ((I & (1 << LV)) == 0) { S.lv[LV] = d; return; } \
  { const float t1 = S.lv[LV] + dppf<CTRL>(S.lv[LV]), t2 = d + dppf<CTRL>(d); d = (c & BIT) ? t2 : t1; }
  TR_LEVEL(0, XOR8, 8) TR_LEVEL(1, XOR7, 4) TR_LEVEL(2, XOR2, 2) TR_LEVEL(3, XOR1, 1)
#undef TR_LEVEL
  S.s[I >> 4] = d;
}
template <int I> __device__ __forceinline__ void eat_v(Stream& S) {
  const unsigned t = dppu<BCAST + rev4(I & 15)>(S.pp[I >> 4]);
  const v4u cf = {t & S.msk[0], t & S.msk[1], t & S.msk[2], t & S.msk[3]};
  S.acc = mfma(cf, S.a[32 + I], S.acc);
}
template <int LO, int HI> __device__ __forceinline__ void eat_ks(Stream& S) { if constexpr (LO < HI) { eat_k<LO>(S); eat_ks<LO + 1, HI>(S); } }
template <int LO, int HI> __device__ __forceinline__ void eat_vs(Stream& S) { if constexpr (LO < HI) { eat_v<LO>(S); SB(); eat_vs<LO + 1, HI>(S); } }

template <int N> __device__ __forceinline__ void stream_step(Stream& S) {
  if constexpr (N < 64) {
    if constexpr (N >= KN_R) wait_vm<KN_R - 1>();
    if constexpr (N < 32) S.a[N] = __builtin_amdgcn_raw_buffer_load_b128(S.rk, S.voff, N * 8192, AUX_NT);
    else S.a[N] = __builtin_amdgcn_raw_buffer_load_b128(S.rv, S.voff, (N - 32) * 8192, AUX_NT);
    SB();
    if constexpr (N == KN_R - 1) barrier();
    if constexpr (N == KN_R) STAMP(1);
    if constexpr (N == KN_N0) {
      STAMP(2);
      lds_wait_ge((LAS unsigned*)(S.L + L_FL) + 1, 1u);
      S.q = *(LAS v4u*)(S.L + L_Q + (S.lane & 15) * 16);
      STAMP(3);
    }
    eat_ks<(N > 0 ? k_done(N - 1) : 0), k_done(N)>(S);
    if constexpr (N == soft_step()) {
      constexpr float C2 = 0.08838834764831845f * 1.4426950408889634f;
      const float s0 = S.s[0] * C2, s1 = S.s[1] * C2;
      const float m = wave_max(fmaxf(s0, s1));
      const float p0 = __builtin_amdgcn_exp2f(s0 - m), p1 = __builtin_amdgcn_exp2f(s1 - m);
      S.pp[0] = pk_bf16(p0, p0); S.pp[1] = pk_bf16(p1, p1);
      const float l = wave_sum(bf_lo(S.pp[0]) + bf_lo(S.pp[1]));
      if (S.lane == 0) *(LAS v2f*)(S.L + L_ST + S.w * 8) = v2f{m, l};
      lds_signal((LAS unsigned*)(S.L + L_FL) + 3, S.lane);
      STAMP(4);
    }
    eat_vs<(N > 0 ? v_done(N - 1) : 0), v_done(N)>(S);
    SB();
    stream_step<N + 1>(S);
  }
}

template <int M, int MN> __device__ __forceinline__ void gemv_rows(const v4u* wv, unsigned od, const unsigned* sel, v4f& acc) {
  if constexpr (M < MN) {
    const unsigned t = dppu<BCAST + M>(od);
    const v4u cf = {__builtin_amdgcn_perm(t, t, sel[0]), __builtin_amdgcn_perm(t, t, sel[1]), __builtin_amdgcn_perm(t, t, sel[2]), __builtin_amdgcn_perm(t, t, sel[3])};
    acc = mfma(cf, wv[M], acc);
    gemv_rows<M + 1, MN>(wv, od, sel, acc);
  }
}
template <int N> __device__ __forceinline__ void weight_loads(v4u* w2v, v4u* wov, rsrc_t r2, rsrc_t ro, int v2, int vo, int s5) {
  if constexpr (N < 22) {
    if constexpr (N >= KN_RW) wait_vm<KN_RW - 1>();
    if constexpr (N < 5) w2v[N] = __builtin_amdgcn_raw_buffer_load_b128(r2, v2, N * 8192, AUX_NT);
    else if constexpr (N == 5) w2v[N] = __builtin_amdgcn_raw_buffer_load_b128(r2, v2, s5, AUX_NT);
    else wov[N - 6] = __builtin_amdgcn_raw_buffer_load_b128(ro, vo, (N - 6) * 8192, AUX_NT);
    SB();
    weight_loads<N + 1>(w2v, wov, r2, ro, v2, vo, s5);
  }
}

__global__ void __launch_bounds__(1024, 4)
fused_kernel(const unsigned char* __restrict__ x, const unsigned char* __restrict__ kc, const unsigned char* __restrict__ vc,
             const unsigned char* __restrict__ wq, const unsigned char* __restrict__ wo, const unsigned char* __restrict__ w1,
             const unsigned char* __restrict__ w3, const unsigned char* __restrict__ w2, const unsigned char* __restrict__ gw,
             unsigned char* out, unsigned char* ws) {
  __shared__ __attribute__((aligned(16))) unsigned char smem[L_BYTES];
  LAS unsigned char* L = (LAS unsigned char*)smem;
  const int tid = threadIdx.x, lane = tid & 63, w16 = __builtin_amdgcn_readfirstlane(tid >> 6), w = w16 & 7;
  const int bid = blockIdx.x, xcd = bid & 7, j = bid >> 3, h = 4 * xcd + (j & 3), b = j >> 2;
  const int r = lane >> 4, c = lane & 15;
  if (w16 >= 8) {
    Stream S;
    const size_t base = ((size_t)(b * NH + h) * SK + (SK - WIN)) * 256;
    S.rk = mk_rsrc(kc + base, WIN * 256); S.rv = mk_rsrc(vc + base, WIN * 256);
    S.voff = lane * 16 + w * 1024; S.lane = lane; S.L = L; S.w = w;
    S.acc = v4f{0.f, 0.f, 0.f, 0.f};
#pragma unroll
    for (int d = 0; d < 4; ++d) S.msk[d] = (c >> 1) == d ? ((c & 1) ? 0xffff0000u : 0x0000ffffu) : 0u;
    stream_step<0>(S);
    eat_vs<v_done(63), 32>(S);
    STAMP(5);
    if (lane < 32) *(LAS v4f*)(L + L_OP + w * 512 + (8 * c + 4 * r) * 4) = S.acc;
    barrier();
  } else {
    const rsrc_t rws = mk_rsrc(ws, WS_BYTES);
    const int t0 = xcd * 86 / 8, nt = (xcd + 1) * 86 / 8 - t0, t = t0 + j - 16;
    const bool isq = j < 16, valid = j < 16 + nt;
    const unsigned char* ub = isq ? wq : ((t & 1) ? w3 : w1);
    const int ustr = isq ? 8192 : 2 * FF, ucb = isq ? (16 * xcd + j) * 64 : (t >> 1) * 64;
    const int ucol = isq ? (16 * xcd + j) * 32 : 4096 + (t & 1) * FF + (t >> 1) * 32;
    const rsrc_t ru = mk_rsrc(ub, valid ? DM * ustr : 0);
    const int vu = (64 * w + (lane >> 2)) * ustr + ucb + (lane & 3) * 16;
    v4u u[4];
#pragma unroll
    for (int i = 0; i < 4; ++i) u[i] = __builtin_amdgcn_raw_buffer_load_b128(ru, vu, i * 16 * ustr, AUX_NT);
    const rsrc_t rx = mk_rsrc(x, 8 * DM * 2);
    const v4u xa0 = __builtin_amdgcn_raw_buffer_load_b128(rx, ((c & 7) * DM + 64 * w + 8 * r) * 2, 0, AUX_NT);
    const v4u xa1 = __builtin_amdgcn_raw_buffer_load_b128(rx, ((c & 7) * DM + 64 * w + 8 * r) * 2, 64, AUX_NT);
    const int fch = (t >> 1) * 4 + ((lane & 31) >> 3);
    const int hcons = isq ? 4 * xcd + (j >> 2) : (8 * fch + 7) / 43;
    const unsigned tag_u = __builtin_amdgcn_raw_buffer_load_b32(rws, valid ? WS_EP + wg_of(w, hcons) * 4 : OOB, 0, AUX_SC1) + TAG_STEP;
    const int hq = 4 * w + ((c & 7) >> 1);
    const unsigned tag_f = __builtin_amdgcn_raw_buffer_load_b32(rws, WS_EP + wg_of(b, hq) * 4, 0, AUX_SC1) + TAG_STEP;
    const unsigned tag_me = __builtin_amdgcn_raw_buffer_load_b32(rws, WS_EP + bid * 4, 0, AUX_SC1) + TAG_STEP;
    v4u gx = {0, 0, 0, 0}, gg = {0, 0, 0, 0};
    if (w == 1) {
      gx = __builtin_amdgcn_raw_buffer_load_b128(rx, b * DM * 2 + lane * 16, 0, AUX_NT);
      gg = __builtin_amdgcn_raw_buffer_load_b128(mk_rsrc(gw, DM * 2), lane * 16, 0, AUX_NT);
    }
    if (tid < 4) ((LAS unsigned*)(L + L_FL))[tid] = 0u;
    barrier();
    LAS unsigned* fl = (LAS unsigned*)(L + L_FL);
    if (valid) {
      LAS unsigned char* stg = L + L_STG + w * 4096;
#pragma unroll
      for (int i = 0; i < 4; ++i) *(LAS v4u*)(stg + i * 1024 + lane * 16) = u[i];
      STAMP(1);
      LAS unsigned char* tr = stg + (8 * r + (c >> 2)) * 64 + (c & 3) * 8;
      v4f ua[2];
#pragma unroll
      for (int tt = 0; tt < 2; ++tt) {
        ua[tt] = v4f{0.f, 0.f, 0.f, 0.f};
#pragma unroll
        for (int s = 0; s < 2; ++s) {
          const v4s lo = __builtin_amdgcn_ds_read_tr16_b64_v4i16((LAS v4s*)(tr + s * 2048 + tt * 32));
          const v4s hi = __builtin_amdgcn_ds_read_tr16_b64_v4i16((LAS v4s*)(tr + s * 2048 + tt * 32 + 256));
          const v2u l2 = __builtin_bit_cast(v2u, lo), h2 = __builtin_bit_cast(v2u, hi);
          ua[tt] = mfma(s ? xa1 : xa0, v4u{l2.x, l2.y, h2.x, h2.y}, ua[tt]);
        }
      }
      if (lane < 32) {
        *(LAS v4f*)(L + L_RED + w * 1024 + lane * 16) = ua[0];
        *(LAS v4f*)(L + L_RED + w * 1024 + 512 + lane * 16) = ua[1];
      }
      lds_signal(fl + 0, lane);
      lds_wait_ge(fl + 0, 8u);
      const int ro_ = L_RED + ((lane >> 4) & 1) * 512 + ((w >> 2) * 16 + c) * 16 + (w & 3) * 4;
      float sum = 0.f;
#pragma unroll
      for (int ww = 0; ww < 8; ++ww) sum += *(LAS float*)(L + ro_ + ww * 1024);
      __builtin_amdgcn_raw_buffer_store_b64(v2u{__float_as_uint(sum), tag_u}, rws, lane < 32 ? WS_PX + (w * NCOL + ucol + lane) * 8 : OOB, 0, AUX_SC1);
      STAMP(2);
    }
    const int c0 = h * 43 / 8, nch = (h + 1) * 43 / 8 - c0;
    if (w == 0) {
      v4u g;
      int po = WS_PX + (b * NCOL + h * 128 + 2 * lane) * 8;
#pragma unroll 1
      for (unsigned spins = 0; spins < SPIN_CAP; ++spins) {
        asm volatile("" : "+v"(po)::"memory");
        g = __builtin_amdgcn_raw_buffer_load_b128(rws, po, 0, AUX_SC1);
        if (__all(g.y == tag_me && g.w == tag_me)) break;
      }
      *(LAS unsigned*)(L + L_Q + lane * 4) = pk_bf16(__uint_as_float(g.x), __uint_as_float(g.z));
      lds_signal(fl + 1, lane);
      STAMP(3);
    }
    if (w == 1) {
      float s = 0.f;
#pragma unroll
      for (int d = 0; d < 4; ++d) s += rbf(bf_lo(gx[d]) * bf_lo(gg[d])) + rbf(bf_hi(gx[d]) * bf_hi(gg[d]));
      s = rbf(wave_sum(s));
      const float g = rbf(1.0f / (1.0f + __expf(-s)));
      if (lane == 0) *(LAS v2f*)(L + L_G) = v2f{g, rbf(1.0f - g)};
      const bool on = lane < 8 * nch;
      int po = on ? WS_PX + (b * NCOL + 4096 + 8 * c0 + lane) * 8 : OOB;
      v2u ga, up;
#pragma unroll 1
      for (unsigned spins = 0; spins < SPIN_CAP; ++spins) {
        asm volatile("" : "+v"(po)::"memory");
        ga = __builtin_amdgcn_raw_buffer_load_b64(rws, po, 0, AUX_SC1);
        up = __builtin_amdgcn_raw_buffer_load_b64(rws, po, FF * 8, AUX_SC1);
        if (__all(!on || (ga.y == tag_me && up.y == tag_me))) break;
      }
      const float gv = rbf(__uint_as_float(ga.x)), uv = rbf(__uint_as_float(up.x));
      const float hv = on ? rbf(gv / (1.0f + __expf(-gv))) * uv : 0.f;
      *(LAS __bf16*)(L + L_H + lane * 2) = (__bf16)hv;
      lds_signal(fl + 2, lane);
      STAMP(3);
    }
    v4u w2v[6], wov[16];
    const int vrow = (2 * r + (c >> 3)) * 1024 + w * 128 + (c & 7) * 16;
    weight_loads<0>(w2v, wov, mk_rsrc(w2, FF * 1024), mk_rsrc(wo, 4096 * 1024), c0 * 8192 + vrow, h * 131072 + vrow, nch == 6 ? 5 * 8192 : OOB);
    unsigned sel[4];
#pragma unroll
    for (int d = 0; d < 4; ++d) {
      const unsigned pick = (c & 8) ? 0x0302u : 0x0100u;
      sel[d] = ((c & 7) >> 1) == d ? ((c & 1) ? (pick << 16) | 0x0c0cu : 0x0c0c0000u | pick) : 0x0c0c0c0cu;
    }
    lds_wait_ge(fl + 2, 1u);
    const unsigned hd = *(LAS unsigned*)(L + L_H + ((4 * c + r) & 31) * 4);
    const v2f gm = *(LAS v2f*)(L + L_G);
    v4f acc = {0.f, 0.f, 0.f, 0.f};
    gemv_rows<0, 6>(w2v, hd, sel, acc);
    acc *= gm.y;
    lds_wait_ge(fl + 3, 8u);
    float fw[8];
    {
      float M = -INFINITY, Ls = 0.f;
      v2f st[8];
#pragma unroll
      for (int ww = 0; ww < 8; ++ww) { st[ww] = *(LAS v2f*)(L + L_ST + ww * 8); M = fmaxf(M, st[ww].x); }
#pragma unroll
      for (int ww = 0; ww < 8; ++ww) { fw[ww] = __builtin_amdgcn_exp2f(st[ww].x - M); Ls = fmaf(fw[ww], st[ww].y, Ls); }
      const float inv = gm.x / Ls;
#pragma unroll
      for (int ww = 0; ww < 8; ++ww) fw[ww] *= inv;
    }
    wait_vm<0>();
    STAMP(4);
    barrier();
    STAMP(5);
    v2f o2 = {0.f, 0.f};
#pragma unroll
    for (int ww = 0; ww < 8; ++ww) { const v2f p = *(LAS v2f*)(L + L_OP + ww * 512 + (8 * c + 2 * r) * 4); o2.x = fmaf(fw[ww], p.x, o2.x); o2.y = fmaf(fw[ww], p.y, o2.y); }
    const unsigned od = pk_bf16(o2.x, o2.y);
    gemv_rows<0, 16>(wov, od, sel, acc);
    {
      auto s0 = __builtin_amdgcn_permlane32_swap(__float_as_uint(acc.x), __float_as_uint(acc.z), false, false);
      auto s1 = __builtin_amdgcn_permlane32_swap(__float_as_uint(acc.y), __float_as_uint(acc.w), false, false);
      const float y0 = __uint_as_float(s0[0]) + dppf<XOR8>(__uint_as_float(s0[1]));
      const float y1 = __uint_as_float(s1[0]) + dppf<XOR8>(__uint_as_float(s1[1]));
      const int fo = WS_FX + ((b * 32 + hq) * 32 + h) * 128 + (8 * (c & 1) + 4 * (r & 1) + 2 * (r >> 1)) * 8;
      __builtin_amdgcn_raw_buffer_store_b128(v4u{__float_as_uint(y0), tag_f, __float_as_uint(y1), tag_f}, rws, c < 8 ? fo : OOB, 0, AUX_SC1);
    }
    STAMP(6);
    if (w == 0) {
      int io = WS_FX + (b * 32 + h) * 4096 + lane * 16;
      v4u g[4];
#pragma unroll 1
      for (unsigned spins = 0; spins < SPIN_CAP; ++spins) {
        asm volatile("" : "+v"(io)::"memory");
        bool ok = true;
#pragma unroll
        for (int i = 0; i < 4; ++i) g[i] = __builtin_amdgcn_raw_buffer_load_b128(rws, io, i * 1024, AUX_SC1);
#pragma unroll
        for (int i = 0; i < 4; ++i) ok = ok && g[i].y == tag_me && g[i].w == tag_me;
        if (__all(ok)) break;
      }
      float y0 = 0.f, y1 = 0.f;
#pragma unroll
      for (int i = 0; i < 4; ++i) { y0 += __uint_as_float(g[i].x); y1 += __uint_as_float(g[i].z); }
      y0 += dppf<XOR8>(y0); y1 += dppf<XOR8>(y1);
      y0 = rows_sum(y0); y1 = rows_sum(y1);
      if (lane < 8) *(GAS unsigned*)(out + (b * DM + 16 * h + 2 * lane) * 2) = pk_bf16(y0, y1);
      __builtin_amdgcn_raw_buffer_store_b32(tag_me, rws, lane == 0 ? WS_EP + bid * 4 : OOB, 0, AUX_SC1);
      STAMP(7);
    }
  }
}

}

extern "C" void kernel_launch(void* const* d_in, const int* in_sizes, int n_in, void* d_out, int out_size, void* d_ws, size_t ws_size,
                              hipStream_t stream) {
  fused_kernel<<<256, 1024, 0, stream>>>((const unsigned char*)d_in[0], (const unsigned char*)d_in[1], (const unsigned char*)d_in[2],
                                         (const unsigned char*)d_in[3], (const unsigned char*)d_in[4], (const unsigned char*)d_in[6],
                                         (const unsigned char*)d_in[7], (const unsigned char*)d_in[8], (const unsigned char*)d_in[9],
                                         (unsigned char*)d_out, (unsigned char*)d_ws);
}
```

```cpp
#define KN_R 4
#define KN_TOUCH 65536
#include <hip/hip_runtime.h>
#include <hip/hip_bf16.h>

#define KN_N0 20
#define KN_RW 2

namespace {

typedef unsigned v4u __attribute__((ext_vector_type(4)));
typedef unsigned v2u __attribute__((ext_vector_type(2)));
typedef float v4f __attribute__((ext_vector_type(4)));
typedef float v2f __attribute__((ext_vector_type(2)));
typedef short v4s __attribute__((ext_vector_type(4)));
typedef __bf16 v8bf __attribute__((ext_vector_type(8)));
typedef __bf16 v2bf __attribute__((ext_vector_type(2)));
#define LAS __attribute__((address_space(3)))
#define GAS __attribute__((address_space(1)))
typedef __amdgpu_buffer_rsrc_t rsrc_t;

constexpr int DM = 512, NH = 32, SK = 4096, WIN = 1024, FF = 1376;
constexpr int NCOL = 4096 + 2 * FF;
constexpr int WS_EP = 0, WS_PX = 4096, WS_FX = 512 * 1024, WS_BYTES = WS_FX + 8 * 32 * 32 * 16 * 8;
constexpr int AUX_NT = 2, AUX_SC1 = 16, OOB = 0x7ffffff0;
constexpr unsigned TAG_STEP = 0x9E3779B9u;
constexpr unsigned SPIN_CAP = 1u << 16;
constexpr int L_STG = 0;
constexpr int L_RED = 32768;
constexpr int L_OP = L_RED + 8192;
constexpr int L_Q = L_OP + 4096;
constexpr int L_H = L_Q + 256;
constexpr int L_ST = L_H + 128;
constexpr int L_G = L_ST + 64;
constexpr int L_FL = L_G + 16;
constexpr int L_BYTES = L_FL + 16;

__device__ __forceinline__ rsrc_t mk_rsrc(const void* p, int bytes) { return __builtin_amdgcn_make_buffer_rsrc((void*)p, 0, bytes, 0x00020000); }
__device__ __forceinline__ unsigned pk_bf16(float lo, float hi) { v2bf r = {(__bf16)lo, (__bf16)hi}; return __builtin_bit_cast(unsigned, r); }
__device__ __forceinline__ float rbf(float x) { return (float)(__bf16)x; }
__device__ __forceinline__ float bf_lo(unsigned u) { return __builtin_bit_cast(float, u << 16); }
__device__ __forceinline__ float bf_hi(unsigned u) { return __builtin_bit_cast(float, u & 0xffff0000u); }
__device__ __forceinline__ float dot2(unsigned a, unsigned b, float c) { return __builtin_amdgcn_fdot2_f32_bf16(__builtin_bit_cast(v2bf, a), __builtin_bit_cast(v2bf, b), c, false); }
__device__ __forceinline__ v4f mfma(v4u a, v4u b, v4f c) { return __builtin_amdgcn_mfma_f32_16x16x32_bf16(__builtin_bit_cast(v8bf, a), __builtin_bit_cast(v8bf, b), c, 0, 0, 0); }
template <int CTRL> __device__ __forceinline__ unsigned dppu(unsigned x) { return (unsigned)__builtin_amdgcn_mov_dpp((int)x, CTRL, 0xf, 0xf, true); }
template <int CTRL> __device__ __forceinline__ float dppf(float x) { return __builtin_bit_cast(float, dppu<CTRL>(__builtin_bit_cast(unsigned, x))); }
constexpr int XOR1 = 0xB1, XOR2 = 0x4E, XOR7 = 0x141, XOR8 = 0x128, BCAST = 0x150;
__device__ __forceinline__ float rows_sum(float x) {
  auto s = __builtin_amdgcn_permlane16_swap(__float_as_uint(x), __float_as_uint(x), false, false);
  x = __uint_as_float(s[0]) + __uint_as_float(s[1]);
  auto t = __builtin_amdgcn_permlane32_swap(__float_as_uint(x), __float_as_uint(x), false, false);
  return __uint_as_float(t[0]) + __uint_as_float(t[1]);
}
__device__ __forceinline__ float rows_max(float x) {
  auto s = __builtin_amdgcn_permlane16_swap(__float_as_uint(x), __float_as_uint(x), false, false);
  x = fmaxf(__uint_as_float(s[0]), __uint_as_float(s[1]));
  auto t = __builtin_amdgcn_permlane32_swap(__float_as_uint(x), __float_as_uint(x), false, false);
  return fmaxf(__uint_as_float(t[0]), __uint_as_float(t[1]));
}
__device__ __forceinline__ float wave_sum(float x) { x += dppf<XOR8>(x); x += dppf<XOR7>(x); x += dppf<XOR2>(x); x += dppf<XOR1>(x); return rows_sum(x); }
__device__ __forceinline__ float wave_max(float x) { x = fmaxf(x, dppf<XOR8>(x)); x = fmaxf(x, dppf<XOR7>(x)); x = fmaxf(x, dppf<XOR2>(x)); x = fmaxf(x, dppf<XOR1>(x)); return rows_max(x); }
template <int N> __device__ __forceinline__ void wait_vm() { asm volatile("s_waitcnt vmcnt(%0)" ::"i"(N) : "memory"); }
__device__ __forceinline__ void wait_lds() { asm volatile("s_waitcnt lgkmcnt(0)" ::: "memory"); }
__device__ __forceinline__ void barrier() { asm volatile("s_waitcnt lgkmcnt(0)\n\ts_barrier" ::: "memory"); }
__device__ __forceinline__ void lds_wait_ge(LAS unsigned* p, unsigned want) {
  while ((unsigned)__builtin_amdgcn_readfirstlane(__hip_atomic_load(p, __ATOMIC_RELAXED, __HIP_MEMORY_SCOPE_WORKGROUP)) < want) __builtin_amdgcn_s_sleep(2);
  asm volatile("" ::: "memory");
}
__device__ __forceinline__ void lds_signal(LAS unsigned* p, int lane) {
  wait_lds();
  if (lane == 0) __hip_atomic_fetch_add(p, 1u, __ATOMIC_RELAXED, __HIP_MEMORY_SCOPE_WORKGROUP);
  asm volatile("" ::: "memory");
}
#define SB() __builtin_amdgcn_sched_barrier(0)
constexpr int rev4(int i) { return ((i & 1) << 3) | ((i & 2) << 1) | ((i & 4) >> 1) | ((i & 8) >> 3); }
__device__ __forceinline__ int wg_of(int b, int h) { return (h >> 2) + 8 * ((h & 3) + 4 * b); }

#define STAMP(i) do {} while (0)

struct Stream {
  rsrc_t rk, rv;
  int voff, lane;
  LAS unsigned char* L;
  v4u a[64];
  v4u q;
  float lv[5];
  unsigned pp[2], msk[4];
  float s[2];
  v4f acc;
  int w;
};
constexpr int k_done(int n) { int d = n < KN_N0 ? 0 : 2 * (n - KN_N0 + 1); int lim = n - KN_R + 1; d = d < lim ? d : lim; return d < 0 ? 0 : (d > 32 ? 32 : d); }
constexpr int soft_step() { int n = 0; while (k_done(n) < 32) ++n; return n + 1; }
constexpr int v_done(int n) { int d = n <= soft_step() ? 0 : 2 * (n - soft_step()); int lim = n - KN_R + 1 - 32; d = d < lim ? d : lim; return d < 0 ? 0 : (d > 32 ? 32 : d); }

template <int I> __device__ __forceinline__ void eat_k(Stream& S) {
  float d = dot2(S.a[I].x, S.q.x, 0.f);
  d = dot2(S.a[I].y, S.q.y, d); d = dot2(S.a[I].z, S.q.z, d); d = dot2(S.a[I].w, S.q.w, d);
  const int c = S.lane;
#define TR_LEVEL(LV, CTRL, BIT) \
  if constexpr ((I & (1 << LV)) == 0) { S.lv[LV] = d; return; } \
  { const float t1 = S.lv[LV] + dppf<CTRL>(S.lv[LV]), t2 = d + dppf<CTRL>(d); d = (c & BIT) ? t2 : t1; }
  TR_LEVEL(0, XOR8, 8) TR_LEVEL(1, XOR7, 4) TR_LEVEL(2, XOR2, 2) TR_LEVEL(3, XOR1, 1)
#undef TR_LEVEL
  S.s[I >> 4] = d;
}
template <int I> __device__ __forceinline__ void eat_v(Stream& S) {
  const unsigned t = dppu<BCAST + rev4(I & 15)>(S.pp[I >> 4]);
  const v4u cf = {t & S.msk[0], t & S.msk[1], t & S.msk[2], t & S.msk[3]};
  S.acc = mfma(cf, S.a[32 + I], S.acc);
}
template <int LO, int HI> __device__ __forceinline__ void eat_ks(Stream& S) { if constexpr (LO < HI) { eat_k<LO>(S); eat_ks<LO + 1, HI>(S); } }
template <int LO, int HI> __device__ __forceinline__ void eat_vs(Stream& S) { if constexpr (LO < HI) { eat_v<LO>(S); SB(); eat_vs<LO + 1, HI>(S); } }

template <int N> __device__ __forceinline__ void stream_step(Stream& S) {
  if constexpr (N < 64) {
    if constexpr (N >= KN_R) wait_vm<KN_R - 1>();
    if constexpr (N < 32) S.a[N] = __builtin_amdgcn_raw_buffer_load_b128(S.rk, S.voff, N * 8192, AUX_NT);
    else S.a[N] = __builtin_amdgcn_raw_buffer_load_b128(S.rv, S.voff, (N - 32) * 8192, AUX_NT);
    SB();
    if constexpr (N == KN_R - 1) barrier();
    if constexpr (N == KN_R) STAMP(1);
    if constexpr (N == KN_N0) {
      STAMP(2);
      lds_wait_ge((LAS unsigned*)(S.L + L_FL) + 1, 1u);
      S.q = *(LAS v4u*)(S.L + L_Q + (S.lane & 15) * 16);
      STAMP(3);
    }
    eat_ks<(N > 0 ? k_done(N - 1) : 0), k_done(N)>(S);
    if constexpr (N == soft_step()) {
      constexpr float C2 = 0.08838834764831845f * 1.4426950408889634f;
      const float s0 = S.s[0] * C2, s1 = S.s[1] * C2;
      const float m = wave_max(fmaxf(s0, s1));
      const float p0 = __builtin_amdgcn_exp2f(s0 - m), p1 = __builtin_amdgcn_exp2f(s1 - m);
      S.pp[0] = pk_bf16(p0, p0); S.pp[1] = pk_bf16(p1, p1);
      const float l = wave_sum(bf_lo(S.pp[0]) + bf_lo(S.pp[1]));
      if (S.lane == 0) *(LAS v2f*)(S.L + L_ST + S.w * 8) = v2f{m, l};
      lds_signal((LAS unsigned*)(S.L + L_FL) + 3, S.lane);
      STAMP(4);
    }
    eat_vs<(N > 0 ? v_done(N - 1) : 0), v_done(N)>(S);
    SB();
    stream_step<N + 1>(S);
  }
}

template <int M, int MN> __device__ __forceinline__ void gemv_rows(const v4u* wv, unsigned od, const unsigned* sel, v4f& acc) {
  if constexpr (M < MN) {
    const unsigned t = dppu<BCAST + M>(od);
    const v4u cf = {__builtin_amdgcn_perm(t, t, sel[0]), __builtin_amdgcn_perm(t, t, sel[1]), __builtin_amdgcn_perm(t, t, sel[2]), __builtin_amdgcn_perm(t, t, sel[3])};
    acc = mfma(cf, wv[M], acc);
    gemv_rows<M + 1, MN>(wv, od, sel, acc);
  }
}
template <int N> __device__ __forceinline__ void weight_loads(v4u* w2v, v4u* wov, rsrc_t r2, rsrc_t ro, int v2, int vo, int s5) {
  if constexpr (N < 22) {
    if constexpr (N >= KN_RW) wait_vm<KN_RW - 1>();
    if constexpr (N < 5) w2v[N] = __builtin_amdgcn_raw_buffer_load_b128(r2, v2, N * 8192, AUX_NT);
    else if constexpr (N == 5) w2v[N] = __builtin_amdgcn_raw_buffer_load_b128(r2, v2, s5, AUX_NT);
    else wov[N - 6] = __builtin_amdgcn_raw_buffer_load_b128(ro, vo, (N - 6) * 8192, AUX_NT);
    SB();
    weight_loads<N + 1>(w2v, wov, r2, ro, v2, vo, s5);
  }
}

__global__ void __launch_bounds__(1024, 4)
fused_kernel(const unsigned char* __restrict__ x, const unsigned char* __restrict__ kc, const unsigned char* __restrict__ vc,
             const unsigned char* __restrict__ wq, const unsigned char* __restrict__ wo, const unsigned char* __restrict__ w1,
             const unsigned char* __restrict__ w3, const unsigned char* __restrict__ w2, const unsigned char* __restrict__ gw,
             unsigned char* out, unsigned char* ws) {
  __shared__ __attribute__((aligned(16))) unsigned char smem[L_BYTES];
  LAS unsigned char* L = (LAS unsigned char*)smem;
  const int tid = threadIdx.x, lane = tid & 63, w16 = __builtin_amdgcn_readfirstlane(tid >> 6), w = w16 & 7;
  const int bid = blockIdx.x, xcd = bid & 7, j = bid >> 3, h = 4 * xcd + (j & 3), b = j >> 2;
  const int r = lane >> 4, c = lane & 15;
  if (w16 >= 8) {
    Stream S;
    const size_t base = ((size_t)(b * NH + h) * SK + (SK - WIN)) * 256;
    S.rk = mk_rsrc(kc + base, WIN * 256); S.rv = mk_rsrc(vc + base, WIN * 256);
    S.voff = lane * 16 + w * 1024; S.lane = lane; S.L = L; S.w = w;
    S.acc = v4f{0.f, 0.f, 0.f, 0.f};
#pragma unroll
    for (int d = 0; d < 4; ++d) S.msk[d] = (c >> 1) == d ? ((c & 1) ? 0xffff0000u : 0x0000ffffu) : 0u;
    stream_step<0>(S);
    eat_vs<v_done(63), 32>(S);
    STAMP(5);
    if (lane < 32) *(LAS v4f*)(L + L_OP + w * 512 + (8 * c + 4 * r) * 4) = S.acc;
    barrier();
  } else {
    const rsrc_t rws = mk_rsrc(ws, WS_BYTES);
    const int t0 = xcd * 86 / 8, nt = (xcd + 1) * 86 / 8 - t0, t = t0 + j - 16;
    const bool isq = j < 16, valid = j < 16 + nt;
    const unsigned char* ub = isq ? wq : ((t & 1) ? w3 : w1);
    const int ustr = isq ? 8192 : 2 * FF, ucb = isq ? (16 * xcd + j) * 64 : (t >> 1) * 64;
    const int ucol = isq ? (16 * xcd + j) * 32 : 4096 + (t & 1) * FF + (t >> 1) * 32;
    const rsrc_t ru = mk_rsrc(ub, valid ? DM * ustr : 0);
    const int vu = (64 * w + (lane >> 2)) * ustr + ucb + (lane & 3) * 16;
    v4u u[4];
#pragma unroll
    for (int i = 0; i < 4; ++i) u[i] = __builtin_amdgcn_raw_buffer_load_b128(ru, vu, i * 16 * ustr, AUX_NT);
    const rsrc_t rx = mk_rsrc(x, 8 * DM * 2);
    const v4u xa0 = __builtin_amdgcn_raw_buffer_load_b128(rx, ((c & 7) * DM + 64 * w + 8 * r) * 2, 0, AUX_NT);
    const v4u xa1 = __builtin_amdgcn_raw_buffer_load_b128(rx, ((c & 7) * DM + 64 * w + 8 * r) * 2, 64, AUX_NT);
    const int fch = (t >> 1) * 4 + ((lane & 31) >> 3);
    const int hcons = isq ? 4 * xcd + (j >> 2) : (8 * fch + 7) / 43;
    const unsigned tag_u = __builtin_amdgcn_raw_buffer_load_b32(rws, valid ? WS_EP + wg_of(w, hcons) * 4 : OOB, 0, AUX_SC1) + TAG_STEP;
    const int hq = 4 * w + ((c & 7) >> 1);
    const unsigned tag_f = __builtin_amdgcn_raw_buffer_load_b32(rws, WS_EP + wg_of(b, hq) * 4, 0, AUX_SC1) + TAG_STEP;
    const unsigned tag_me = __builtin_amdgcn_raw_buffer_load_b32(rws, WS_EP + bid * 4, 0, AUX_SC1) + TAG_STEP;
    unsigned touch = 0;
    if (w == 7) {
      const size_t base = ((size_t)(b * NH + h) * SK + (SK - WIN)) * 256;
      touch = __builtin_amdgcn_raw_buffer_load_b32(mk_rsrc(kc + base, WIN * 256), lane * KN_TOUCH, 0, AUX_NT) ^ __builtin_amdgcn_raw_buffer_load_b32(mk_rsrc(vc + base, WIN * 256), lane * KN_TOUCH, 0, AUX_NT);
    }
    v4u gx = {0, 0, 0, 0}, gg = {0, 0, 0, 0};
    if (w == 1) {
      gx = __builtin_amdgcn_raw_buffer_load_b128(rx, b * DM * 2 + lane * 16, 0, AUX_NT);
      gg = __builtin_amdgcn_raw_buffer_load_b128(mk_rsrc(gw, DM * 2), lane * 16, 0, AUX_NT);
    }
    if (tid < 4) ((LAS unsigned*)(L + L_FL))[tid] = 0u;
    barrier();
    LAS unsigned* fl = (LAS unsigned*)(L + L_FL);
    if (valid) {
      LAS unsigned char* stg = L + L_STG + w * 4096;
#pragma unroll
      for (int i = 0; i < 4; ++i) *(LAS v4u*)(stg + i * 1024 + lane * 16) = u[i];
      STAMP(1);
      LAS unsigned char* tr = stg + (8 * r + (c >> 2)) * 64 + (c & 3) * 8;
      v4f ua[2];
#pragma unroll
      for (int tt = 0; tt < 2; ++tt) {
        ua[tt] = v4f{0.f, 0.f, 0.f, 0.f};
#pragma unroll
        for (int s = 0; s < 2; ++s) {
          const v4s lo = __builtin_amdgcn_ds_read_tr16_b64_v4i16((LAS v4s*)(tr + s * 2048 + tt * 32));
          const v4s hi = __builtin_amdgcn_ds_read_tr16_b64_v4i16((LAS v4s*)(tr + s * 2048 + tt * 32 + 256));
          const v2u l2 = __builtin_bit_cast(v2u, lo), h2 = __builtin_bit_cast(v2u, hi);
          ua[tt] = mfma(s ? xa1 : xa0, v4u{l2.x, l2.y, h2.x, h2.y}, ua[tt]);
        }
      }
      if (lane < 32) {
        *(LAS v4f*)(L + L_RED + w * 1024 + lane * 16) = ua[0];
        *(LAS v4f*)(L + L_RED + w * 1024 + 512 + lane * 16) = ua[1];
      }
      lds_signal(fl + 0, lane);
      lds_wait_ge(fl + 0, 8u);
      const int ro_ = L_RED + ((lane >> 4) & 1) * 512 + ((w >> 2) * 16 + c) * 16 + (w & 3) * 4;
      float sum = 0.f;
#pragma unroll
      for (int ww = 0; ww < 8; ++ww) sum += *(LAS float*)(L + ro_ + ww * 1024);
      __builtin_amdgcn_raw_buffer_store_b64(v2u{__float_as_uint(sum), tag_u}, rws, lane < 32 ? WS_PX + (w * NCOL + ucol + lane) * 8 : OOB, 0, AUX_SC1);
      STAMP(2);
    }
    asm volatile("" ::"v"(touch));
    const int c0 = h * 43 / 8, nch = (h + 1) * 43 / 8 - c0;
    if (w == 0) {
      v4u g;
      int po = WS_PX + (b * NCOL + h * 128 + 2 * lane) * 8;
#pragma unroll 1
      for (unsigned spins = 0; spins < SPIN_CAP; ++spins) {
        asm volatile("" : "+v"(po)::"memory");
        g = __builtin_amdgcn_raw_buffer_load_b128(rws, po, 0, AUX_SC1);
        if (__all(g.y == tag_me && g.w == tag_me)) break;
      }
      *(LAS unsigned*)(L + L_Q + lane * 4) = pk_bf16(__uint_as_float(g.x), __uint_as_float(g.z));
      lds_signal(fl + 1, lane);
      STAMP(3);
    }
    if (w == 1) {
      float s = 0.f;
#pragma unroll
      for (int d = 0; d < 4; ++d) s += rbf(bf_lo(gx[d]) * bf_lo(gg[d])) + rbf(bf_hi(gx[d]) * bf_hi(gg[d]));
      s = rbf(wave_sum(s));
      const float g = rbf(1.0f / (1.0f + __expf(-s)));
      if (lane == 0) *(LAS v2f*)(L + L_G) = v2f{g, rbf(1.0f - g)};
      const bool on = lane < 8 * nch;
      int po = on ? WS_PX + (b * NCOL + 4096 + 8 * c0 + lane) * 8 : OOB;
      v2u ga, up;
#pragma unroll 1
      for (unsigned spins = 0; spins < SPIN_CAP; ++spins) {
        asm volatile("" : "+v"(po)::"memory");
        ga = __builtin_amdgcn_raw_buffer_load_b64(rws, po, 0, AUX_SC1);
        up = __builtin_amdgcn_raw_buffer_load_b64(rws, po, FF * 8, AUX_SC1);
        if (__all(!on || (ga.y == tag_me && up.y == tag_me))) break;
      }
      const float gv = rbf(__uint_as_float(ga.x)), uv = rbf(__uint_as_float(up.x));
      const float hv = on ? rbf(gv / (1.0f + __expf(-gv))) * uv : 0.f;
      *(LAS __bf16*)(L + L_H + lane * 2) = (__bf16)hv;
      lds_signal(fl + 2, lane);
      STAMP(3);
    }
    v4u w2v[6], wov[16];
    const int vrow = (2 * r + (c >> 3)) * 1024 + w * 128 + (c & 7) * 16;
    weight_loads<0>(w2v, wov, mk_rsrc(w2, FF * 1024), mk_rsrc(wo, 4096 * 1024), c0 * 8192 + vrow, h * 131072 + vrow, nch == 6 ? 5 * 8192 : OOB);
    unsigned sel[4];
#pragma unroll
    for (int d = 0; d < 4; ++d) {
      const unsigned pick = (c & 8) ? 0x0302u : 0x0100u;
      sel[d] = ((c & 7) >> 1) == d ? ((c & 1) ? (pick << 16) | 0x0c0cu : 0x0c0c0000u | pick) : 0x0c0c0c0cu;
    }
    lds_wait_ge(fl + 2, 1u);
    const unsigned hd = *(LAS unsigned*)(L + L_H + ((4 * c + r) & 31) * 4);
    const v2f gm = *(LAS v2f*)(L + L_G);
    v4f acc = {0.f, 0.f, 0.f, 0.f};
    gemv_rows<0, 6>(w2v, hd, sel, acc);
    acc *= gm.y;
    lds_wait_ge(fl + 3, 8u);
    float fw[8];
    {
      float M = -INFINITY, Ls = 0.f;
      v2f st[8];
#pragma unroll
      for (int ww = 0; ww < 8; ++ww) { st[ww] = *(LAS v2f*)(L + L_ST + ww * 8); M = fmaxf(M, st[ww].x); }
#pragma unroll
      for (int ww = 0; ww < 8; ++ww) { fw[ww] = __builtin_amdgcn_exp2f(st[ww].x - M); Ls = fmaf(fw[ww], st[ww].y, Ls); }
      const float inv = gm.x / Ls;
#pragma unroll
      for (int ww = 0; ww < 8; ++ww) fw[ww] *= inv;
    }
    wait_vm<0>();
    STAMP(4);
    barrier();
    STAMP(5);
    v2f o2 = {0.f, 0.f};
#pragma unroll
    for (int ww = 0; ww < 8; ++ww) { const v2f p = *(LAS v2f*)(L + L_OP + ww * 512 + (8 * c + 2 * r) * 4); o2.x = fmaf(fw[ww], p.x, o2.x); o2.y = fmaf(fw[ww], p.y, o2.y); }
    const unsigned od = pk_bf16(o2.x, o2.y);
    gemv_rows<0, 16>(wov, od, sel, acc);
    {
      auto s0 = __builtin_amdgcn_permlane32_swap(__float_as_uint(acc.x), __float_as_uint(acc.z), false, false);
      auto s1 = __builtin_amdgcn_permlane32_swap(__float_as_uint(acc.y), __float_as_uint(acc.w), false, false);
      const float y0 = __uint_as_float(s0[0]) + dppf<XOR8>(__uint_as_float(s0[1]));
      const float y1 = __uint_as_float(s1[0]) + dppf<XOR8>(__uint_as_float(s1[1]));
      const int fo = WS_FX + ((b * 32 + hq) * 32 + h) * 128 + (8 * (c & 1) + 4 * (r & 1) + 2 * (r >> 1)) * 8;
      __builtin_amdgcn_raw_buffer_store_b128(v4u{__float_as_uint(y0), tag_f, __float_as_uint(y1), tag_f}, rws, c < 8 ? fo : OOB, 0, AUX_SC1);
    }
    STAMP(6);
    if (w == 0) {
      int io = WS_FX + (b * 32 + h) * 4096 + lane * 16;
      v4u g[4];
#pragma unroll 1
      for (unsigned spins = 0; spins < SPIN_CAP; ++spins) {
        asm volatile("" : "+v"(io)::"memory");
        bool ok = true;
#pragma unroll
        for (int i = 0; i < 4; ++i) g[i] = __builtin_amdgcn_raw_buffer_load_b128(rws, io, i * 1024, AUX_SC1);
#pragma unroll
        for (int i = 0; i < 4; ++i) ok = ok && g[i].y == tag_me && g[i].w == tag_me;
        if (__all(ok)) break;
      }
      float y0 = 0.f, y1 = 0.f;
#pragma unroll
      for (int i = 0; i < 4; ++i) { y0 += __uint_as_float(g[i].x); y1 += __uint_as_float(g[i].z); }
      y0 += dppf<XOR8>(y0); y1 += dppf<XOR8>(y1);
      y0 = rows_sum(y0); y1 = rows_sum(y1);
      if (lane < 8) *(GAS unsigned*)(out + (b * DM + 16 * h + 2 * lane) * 2) = pk_bf16(y0, y1);
      __builtin_amdgcn_raw_buffer_store_b32(tag_me, rws, lane == 0 ? WS_EP + bid * 4 : OOB, 0, AUX_SC1);
      STAMP(7);
    }
  }
}

}

extern "C" void kernel_launch(void* const* d_in, const int* in_sizes, int n_in, void* d_out, int out_size, void* d_ws, size_t ws_size,
                              hipStream_t stream) {
  fused_kernel<<<256, 1024, 0, stream>>>((const unsigned char*)d_in[0], (const unsigned char*)d_in[1], (const unsigned char*)d_in[2],
                                         (const unsigned char*)d_in[3], (const unsigned char*)d_in[4], (const unsigned char*)d_in[6],
                                         (const unsigned char*)d_in[7], (const unsigned char*)d_in[8], (const unsigned char*)d_in[9],
                                         (unsigned char*)d_out, (unsigned char*)d_ws);
}
```

```cpp
#define STAMPS
#define KN_R 4
#define DIAG_W 8
#define DIAG_A 0
#define DIAG_B 0
#define DIAG_BID 77
#include <hip/hip_runtime.h>
#include <hip/hip_bf16.h>

#define KN_N0 20
#define KN_RW 2

namespace {

typedef unsigned v4u __attribute__((ext_vector_type(4)));
typedef unsigned v2u __attribute__((ext_vector_type(2)));
typedef float v4f __attribute__((ext_vector_type(4)));
typedef float v2f __attribute__((ext_vector_type(2)));
typedef short v4s __attribute__((ext_vector_type(4)));
typedef __bf16 v8bf __attribute__((ext_vector_type(8)));
typedef __bf16 v2bf __attribute__((ext_vector_type(2)));
#define LAS __attribute__((address_space(3)))
#define GAS __attribute__((address_space(1)))
typedef __amdgpu_buffer_rsrc_t rsrc_t;

constexpr int DM = 512, NH = 32, SK = 4096, WIN = 1024, FF = 1376;
constexpr int NCOL = 4096 + 2 * FF;
constexpr int WS_EP = 0, WS_PX = 4096, WS_FX = 512 * 1024, WS_BYTES = WS_FX + 8 * 32 * 32 * 16 * 8;
constexpr int AUX_NT = 2, AUX_SC1 = 16, OOB = 0x7ffffff0;
constexpr unsigned TAG_STEP = 0x9E3779B9u;
constexpr unsigned SPIN_CAP = 1u << 16;
constexpr int L_STG = 0;
constexpr int L_RED = 32768;
constexpr int L_OP = L_RED + 8192;
constexpr int L_Q = L_OP + 4096;
constexpr int L_H = L_Q + 256;
constexpr int L_ST = L_H + 128;
constexpr int L_G = L_ST + 64;
constexpr int L_FL = L_G + 16;
constexpr int L_BYTES = L_FL + 16;

__device__ __forceinline__ rsrc_t mk_rsrc(const void* p, int bytes) { return __builtin_amdgcn_make_buffer_rsrc((void*)p, 0, bytes, 0x00020000); }
__device__ __forceinline__ unsigned pk_bf16(float lo, float hi) { v2bf r = {(__bf16)lo, (__bf16)hi}; return __builtin_bit_cast(unsigned, r); }
__device__ __forceinline__ float rbf(float x) { return (float)(__bf16)x; }
__device__ __forceinline__ float bf_lo(unsigned u) { return __builtin_bit_cast(float, u << 16); }
__device__ __forceinline__ float bf_hi(unsigned u) { return __builtin_bit_cast(float, u & 0xffff0000u); }
__device__ __forceinline__ float dot2(unsigned a, unsigned b, float c) { return __builtin_amdgcn_fdot2_f32_bf16(__builtin_bit_cast(v2bf, a), __builtin_bit_cast(v2bf, b), c, false); }
__device__ __forceinline__ v4f mfma(v4u a, v4u b, v4f c) { return __builtin_amdgcn_mfma_f32_16x16x32_bf16(__builtin_bit_cast(v8bf, a), __builtin_bit_cast(v8bf, b), c, 0, 0, 0); }
template <int CTRL> __device__ __forceinline__ unsigned dppu(unsigned x) { return (unsigned)__builtin_amdgcn_mov_dpp((int)x, CTRL, 0xf, 0xf, true); }
template <int CTRL> __device__ __forceinline__ float dppf(float x) { return __builtin_bit_cast(float, dppu<CTRL>(__builtin_bit_cast(unsigned, x))); }
constexpr int XOR1 = 0xB1, XOR2 = 0x4E, XOR7 = 0x141, XOR8 = 0x128, BCAST = 0x150;
__device__ __forceinline__ float rows_sum(float x) {
  auto s = __builtin_amdgcn_permlane16_swap(__float_as_uint(x), __float_as_uint(x), false, false);
  x = __uint_as_float(s[0]) + __uint_as_float(s[1]);
  auto t = __builtin_amdgcn_permlane32_swap(__float_as_uint(x), __float_as_uint(x), false, false);
  return __uint_as_float(t[0]) + __uint_as_float(t[1]);
}
__device__ __forceinline__ float rows_max(float x) {
  auto s = __builtin_amdgcn_permlane16_swap(__float_as_uint(x), __float_as_uint(x), false, false);
  x = fmaxf(__uint_as_float(s[0]), __uint_as_float(s[1]));
  auto t = __builtin_amdgcn_permlane32_swap(__float_as_uint(x), __float_as_uint(x), false, false);
  return fmaxf(__uint_as_float(t[0]), __uint_as_float(t[1]));
}
__device__ __forceinline__ float wave_sum(float x) { x += dppf<XOR8>(x); x += dppf<XOR7>(x); x += dppf<XOR2>(x); x += dppf<XOR1>(x); return rows_sum(x); }
__device__ __forceinline__ float wave_max(float x) { x = fmaxf(x, dppf<XOR8>(x)); x = fmaxf(x, dppf<XOR7>(x)); x = fmaxf(x, dppf<XOR2>(x)); x = fmaxf(x, dppf<XOR1>(x)); return rows_max(x); }
template <int N> __device__ __forceinline__ void wait_vm() { asm volatile("s_waitcnt vmcnt(%0)" ::"i"(N) : "memory"); }
__device__ __forceinline__ void wait_lds() { asm volatile("s_waitcnt lgkmcnt(0)" ::: "memory"); }
__device__ __forceinline__ void barrier() { asm volatile("s_waitcnt lgkmcnt(0)\n\ts_barrier" ::: "memory"); }
__device__ __forceinline__ void lds_wait_ge(LAS unsigned* p, unsigned want) {
  while ((unsigned)__builtin_amdgcn_readfirstlane(__hip_atomic_load(p, __ATOMIC_RELAXED, __HIP_MEMORY_SCOPE_WORKGROUP)) < want) __builtin_amdgcn_s_sleep(2);
  asm volatile("" ::: "memory");
}
__device__ __forceinline__ void lds_signal(LAS unsigned* p, int lane) {
  wait_lds();
  if (lane == 0) __hip_atomic_fetch_add(p, 1u, __ATOMIC_RELAXED, __HIP_MEMORY_SCOPE_WORKGROUP);
  asm volatile("" ::: "memory");
}
#define SB() __builtin_amdgcn_sched_barrier(0)
constexpr int rev4(int i) { return ((i & 1) << 3) | ((i & 2) << 1) | ((i & 4) >> 1) | ((i & 8) >> 3); }
__device__ __forceinline__ int wg_of(int b, int h) { return (h >> 2) + 8 * ((h & 3) + 4 * b); }

#define STAMP(i) do { const unsigned t_ = (unsigned)__builtin_amdgcn_s_memrealtime(); stamp = (lane == (i)) ? t_ : stamp; } while (0)

struct Stream {
  rsrc_t rk, rv;
  int voff, lane;
  LAS unsigned char* L;
  v4u a[64];
  v4u q;
  float lv[5];
  unsigned pp[2], msk[4];
  float s[2];
  v4f acc;
  int w;
  unsigned stamp;
};
constexpr int k_done(int n) { int d = n < KN_N0 ? 0 : 2 * (n - KN_N0 + 1); int lim = n - KN_R + 1; d = d < lim ? d : lim; return d < 0 ? 0 : (d > 32 ? 32 : d); }
constexpr int soft_step() { int n = 0; while (k_done(n) < 32) ++n; return n + 1; }
constexpr int v_done(int n) { int d = n <= soft_step() ? 0 : 2 * (n - soft_step()); int lim = n - KN_R + 1 - 32; d = d < lim ? d : lim; return d < 0 ? 0 : (d > 32 ? 32 : d); }

template <int I> __device__ __forceinline__ void eat_k(Stream& S) {
  float d = dot2(S.a[I].x, S.q.x, 0.f);
  d = dot2(S.a[I].y, S.q.y, d); d = dot2(S.a[I].z, S.q.z, d); d = dot2(S.a[I].w, S.q.w, d);
  const int c = S.lane;
#define TR_LEVEL(LV, CTRL, BIT) \
  if constexpr ((I & (1 << LV)) == 0) { S.lv[LV] = d; return; } \
  { const float t1 = S.lv[LV] + dppf<CTRL>(S.lv[LV]), t2 = d + dppf<CTRL>(d); d = (c & BIT) ? t2 : t1; }
  TR_LEVEL(0, XOR8, 8) TR_LEVEL(1, XOR7, 4) TR_LEVEL(2, XOR2, 2) TR_LEVEL(3, XOR1, 1)
#undef TR_LEVEL
  S.s[I >> 4] = d;
}
template <int I> __device__ __forceinline__ void eat_v(Stream& S) {
  const unsigned t = dppu<BCAST + rev4(I & 15)>(S.pp[I >> 4]);
  const v4u cf = {t & S.msk[0], t & S.msk[1], t & S.msk[2], t & S.msk[3]};
  S.acc = mfma(cf, S.a[32 + I], S.acc);
}
template <int LO, int HI> __device__ __forceinline__ void eat_ks(Stream& S) { if constexpr (LO < HI) { eat_k<LO>(S); eat_ks<LO + 1, HI>(S); } }
template <int LO, int HI> __device__ __forceinline__ void eat_vs(Stream& S) { if constexpr (LO < HI) { eat_v<LO>(S); SB(); eat_vs<LO + 1, HI>(S); } }

template <int N> __device__ __forceinline__ void stream_step(Stream& S) {
  if constexpr (N < 64) {
    unsigned& stamp = S.stamp; const int lane = S.lane;
    if constexpr (N >= KN_R) wait_vm<KN_R - 1>();
    if constexpr (N < 32) S.a[N] = __builtin_amdgcn_raw_buffer_load_b128(S.rk, S.voff, N * 8192, AUX_NT);
    else S.a[N] = __builtin_amdgcn_raw_buffer_load_b128(S.rv, S.voff, (N - 32) * 8192, AUX_NT);
    SB();
    if constexpr (N == KN_R - 1) barrier();
    if constexpr (N == KN_R) STAMP(1);
    if constexpr (N == KN_N0) {
      STAMP(2);
      lds_wait_ge((LAS unsigned*)(S.L + L_FL) + 1, 1u);
      S.q = *(LAS v4u*)(S.L + L_Q + (S.lane & 15) * 16);
      STAMP(3);
    }
    eat_ks<(N > 0 ? k_done(N - 1) : 0), k_done(N)>(S);
    if constexpr (N == soft_step()) {
      constexpr float C2 = 0.08838834764831845f * 1.4426950408889634f;
      const float s0 = S.s[0] * C2, s1 = S.s[1] * C2;
      const float m = wave_max(fmaxf(s0, s1));
      const float p0 = __builtin_amdgcn_exp2f(s0 - m), p1 = __builtin_amdgcn_exp2f(s1 - m);
      S.pp[0] = pk_bf16(p0, p0); S.pp[1] = pk_bf16(p1, p1);
      const float l = wave_sum(bf_lo(S.pp[0]) + bf_lo(S.pp[1]));
      if (S.lane == 0) *(LAS v2f*)(S.L + L_ST + S.w * 8) = v2f{m, l};
      lds_signal((LAS unsigned*)(S.L + L_FL) + 3, S.lane);
      STAMP(4);
    }
    eat_vs<(N > 0 ? v_done(N - 1) : 0), v_done(N)>(S);
    SB();
    stream_step<N + 1>(S);
  }
}

template <int M, int MN> __device__ __forceinline__ void gemv_rows(const v4u* wv, unsigned od, const unsigned* sel, v4f& acc) {
  if constexpr (M < MN) {
    const unsigned t = dppu<BCAST + M>(od);
    const v4u cf = {__builtin_amdgcn_perm(t, t, sel[0]), __builtin_amdgcn_perm(t, t, sel[1]), __builtin_amdgcn_perm(t, t, sel[2]), __builtin_amdgcn_perm(t, t, sel[3])};
    acc = mfma(cf, wv[M], acc);
    gemv_rows<M + 1, MN>(wv, od, sel, acc);
  }
}
template <int N> __device__ __forceinline__ void weight_loads(v4u* w2v, v4u* wov, rsrc_t r2, rsrc_t ro, int v2, int vo, int s5) {
  if constexpr (N < 22) {
    if constexpr (N >= KN_RW) wait_vm<KN_RW - 1>();
    if constexpr (N < 5) w2v[N] = __builtin_amdgcn_raw_buffer_load_b128(r2, v2, N * 8192, AUX_NT);
    else if constexpr (N == 5) w2v[N] = __builtin_amdgcn_raw_buffer_load_b128(r2, v2, s5, AUX_NT);
    else wov[N - 6] = __builtin_amdgcn_raw_buffer_load_b128(ro, vo, (N - 6) * 8192, AUX_NT);
    SB();
    weight_loads<N + 1>(w2v, wov, r2, ro, v2, vo, s5);
  }
}

__global__ void __launch_bounds__(1024, 4)
fused_kernel(const unsigned char* __restrict__ x, const unsigned char* __restrict__ kc, const unsigned char* __restrict__ vc,
             const unsigned char* __restrict__ wq, const unsigned char* __restrict__ wo, const unsigned char* __restrict__ w1,
             const unsigned char* __restrict__ w3, const unsigned char* __restrict__ w2, const unsigned char* __restrict__ gw,
             unsigned char* out, unsigned char* ws) {
  __shared__ __attribute__((aligned(16))) unsigned char smem[L_BYTES];
  LAS unsigned char* L = (LAS unsigned char*)smem;
  const int tid = threadIdx.x, lane = tid & 63, w16 = __builtin_amdgcn_readfirstlane(tid >> 6), w = w16 & 7;
  const int bid = blockIdx.x, xcd = bid & 7, j = bid >> 3, h = 4 * xcd + (j & 3), b = j >> 2;
  const int r = lane >> 4, c = lane & 15;
  unsigned stamp = 0;
  STAMP(0);
  if (w16 >= 8) {
    Stream S;
    const size_t base = ((size_t)(b * NH + h) * SK + (SK - WIN)) * 256;
    S.rk = mk_rsrc(kc + base, WIN * 256); S.rv = mk_rsrc(vc + base, WIN * 256);
    S.voff = lane * 16 + w * 1024; S.lane = lane; S.L = L; S.w = w;
    S.acc = v4f{0.f, 0.f, 0.f, 0.f};
#pragma unroll
    for (int d = 0; d < 4; ++d) S.msk[d] = (c >> 1) == d ? ((c & 1) ? 0xffff0000u : 0x0000ffffu) : 0u;
    S.stamp = stamp;
    stream_step<0>(S);
    eat_vs<v_done(63), 32>(S);
    stamp = S.stamp;
    STAMP(5);
    if (lane < 32) *(LAS v4f*)(L + L_OP + w * 512 + (8 * c + 4 * r) * 4) = S.acc;
    barrier();
  } else {
    const rsrc_t rws = mk_rsrc(ws, WS_BYTES);
    const int t0 = xcd * 86 / 8, nt = (xcd + 1) * 86 / 8 - t0, t = t0 + j - 16;
    const bool isq = j < 16, valid = j < 16 + nt;
    const unsigned char* ub = isq ? wq : ((t & 1) ? w3 : w1);
    const int ustr = isq ? 8192 : 2 * FF, ucb = isq ? (16 * xcd + j) * 64 : (t >> 1) * 64;
    const int ucol = isq ? (16 * xcd + j) * 32 : 4096 + (t & 1) * FF + (t >> 1) * 32;
    const rsrc_t ru = mk_rsrc(ub, valid ? DM * ustr : 0);
    const int vu = (64 * w + (lane >> 2)) * ustr + ucb + (lane & 3) * 16;
    v4u u[4];
#pragma unroll
    for (int i = 0; i < 4; ++i) u[i] = __builtin_amdgcn_raw_buffer_load_b128(ru, vu, i * 16 * ustr, AUX_NT);
    const rsrc_t rx = mk_rsrc(x, 8 * DM * 2);
    const v4u xa0 = __builtin_amdgcn_raw_buffer_load_b128(rx, ((c & 7) * DM + 64 * w + 8 * r) * 2, 0, AUX_NT);
    const v4u xa1 = __builtin_amdgcn_raw_buffer_load_b128(rx, ((c & 7) * DM + 64 * w + 8 * r) * 2, 64, AUX_NT);
    const int fch = (t >> 1) * 4 + ((lane & 31) >> 3);
    const int hcons = isq ? 4 * xcd + (j >> 2) : (8 * fch + 7) / 43;
    const unsigned tag_u = __builtin_amdgcn_raw_buffer_load_b32(rws, valid ? WS_EP + wg_of(w, hcons) * 4 : OOB, 0, AUX_SC1) + TAG_STEP;
    const int hq = 4 * w + ((c & 7) >> 1);
    const unsigned tag_f = __builtin_amdgcn_raw_buffer_load_b32(rws, WS_EP + wg_of(b, hq) * 4, 0, AUX_SC1) + TAG_STEP;
    const unsigned tag_me = __builtin_amdgcn_raw_buffer_load_b32(rws, WS_EP + bid * 4, 0, AUX_SC1) + TAG_STEP;
    v4u gx = {0, 0, 0, 0}, gg = {0, 0, 0, 0};
    if (w == 1) {
      gx = __builtin_amdgcn_raw_buffer_load_b128(rx, b * DM * 2 + lane * 16, 0, AUX_NT);
      gg = __builtin_amdgcn_raw_buffer_load_b128(mk_rsrc(gw, DM * 2), lane * 16, 0, AUX_NT);
    }
    if (tid < 4) ((LAS unsigned*)(L + L_FL))[tid] = 0u;
    barrier();
    LAS unsigned* fl = (LAS unsigned*)(L + L_FL);
    if (valid) {
      LAS unsigned char* stg = L + L_STG + w * 4096;
#pragma unroll
      for (int i = 0; i < 4; ++i) *(LAS v4u*)(stg + i * 1024 + lane * 16) = u[i];
      STAMP(1);
      LAS unsigned char* tr = stg + (8 * r + (c >> 2)) * 64 + (c & 3) * 8;
      v4f ua[2];
#pragma unroll
      for (int tt = 0; tt < 2; ++tt) {
        ua[tt] = v4f{0.f, 0.f, 0.f, 0.f};
#pragma unroll
        for (int s = 0; s < 2; ++s) {
          const v4s lo = __builtin_amdgcn_ds_read_tr16_b64_v4i16((LAS v4s*)(tr + s * 2048 + tt * 32));
          const v4s hi = __builtin_amdgcn_ds_read_tr16_b64_v4i16((LAS v4s*)(tr + s * 2048 + tt * 32 + 256));
          const v2u l2 = __builtin_bit_cast(v2u, lo), h2 = __builtin_bit_cast(v2u, hi);
          ua[tt] = mfma(s ? xa1 : xa0, v4u{l2.x, l2.y, h2.x, h2.y}, ua[tt]);
        }
      }
      if (lane < 32) {
        *(LAS v4f*)(L + L_RED + w * 1024 + lane * 16) = ua[0];
        *(LAS v4f*)(L + L_RED + w * 1024 + 512 + lane * 16) = ua[1];
      }
      lds_signal(fl + 0, lane);
      lds_wait_ge(fl + 0, 8u);
      const int ro_ = L_RED + ((lane >> 4) & 1) * 512 + ((w >> 2) * 16 + c) * 16 + (w & 3) * 4;
      float sum = 0.f;
#pragma unroll
      for (int ww = 0; ww < 8; ++ww) sum += *(LAS float*)(L + ro_ + ww * 1024);
      __builtin_amdgcn_raw_buffer_store_b64(v2u{__float_as_uint(sum), tag_u}, rws, lane < 32 ? WS_PX + (w * NCOL + ucol + lane) * 8 : OOB, 0, AUX_SC1);
      STAMP(2);
    }
    const int c0 = h * 43 / 8, nch = (h + 1) * 43 / 8 - c0;
    if (w == 0) {
      v4u g;
      int po = WS_PX + (b * NCOL + h * 128 + 2 * lane) * 8;
#pragma unroll 1
      for (unsigned spins = 0; spins < SPIN_CAP; ++spins) {
        asm volatile("" : "+v"(po)::"memory");
        g = __builtin_amdgcn_raw_buffer_load_b128(rws, po, 0, AUX_SC1);
        if (__all(g.y == tag_me && g.w == tag_me)) break;
      }
      *(LAS unsigned*)(L + L_Q + lane * 4) = pk_bf16(__uint_as_float(g.x), __uint_as_float(g.z));
      lds_signal(fl + 1, lane);
      STAMP(3);
    }
    if (w == 1) {
      float s = 0.f;
#pragma unroll
      for (int d = 0; d < 4; ++d) s += rbf(bf_lo(gx[d]) * bf_lo(gg[d])) + rbf(bf_hi(gx[d]) * bf_hi(gg[d]));
      s = rbf(wave_sum(s));
      const float g = rbf(1.0f / (1.0f + __expf(-s)));
      if (lane == 0) *(LAS v2f*)(L + L_G) = v2f{g, rbf(1.0f - g)};
      const bool on = lane < 8 * nch;
      int po = on ? WS_PX + (b * NCOL + 4096 + 8 * c0 + lane) * 8 : OOB;
      v2u ga, up;
#pragma unroll 1
      for (unsigned spins = 0; spins < SPIN_CAP; ++spins) {
        asm volatile("" : "+v"(po)::"memory");
        ga = __builtin_amdgcn_raw_buffer_load_b64(rws, po, 0, AUX_SC1);
        up = __builtin_amdgcn_raw_buffer_load_b64(rws, po, FF * 8, AUX_SC1);
        if (__all(!on || (ga.y == tag_me && up.y == tag_me))) break;
      }
      const float gv = rbf(__uint_as_float(ga.x)), uv = rbf(__uint_as_float(up.x));
      const float hv = on ? rbf(gv / (1.0f + __expf(-gv))) * uv : 0.f;
      *(LAS __bf16*)(L + L_H + lane * 2) = (__bf16)hv;
      lds_signal(fl + 2, lane);
      STAMP(3);
    }
    v4u w2v[6], wov[16];
    const int vrow = (2 * r + (c >> 3)) * 1024 + w * 128 + (c & 7) * 16;
    weight_loads<0>(w2v, wov, mk_rsrc(w2, FF * 1024), mk_rsrc(wo, 4096 * 1024), c0 * 8192 + vrow, h * 131072 + vrow, nch == 6 ? 5 * 8192 : OOB);
    unsigned sel[4];
#pragma unroll
    for (int d = 0; d < 4; ++d) {
      const unsigned pick = (c & 8) ? 0x0302u : 0x0100u;
      sel[d] = ((c & 7) >> 1) == d ? ((c & 1) ? (pick << 16) | 0x0c0cu : 0x0c0c0000u | pick) : 0x0c0c0c0cu;
    }
    lds_wait_ge(fl + 2, 1u);
    const unsigned hd = *(LAS unsigned*)(L + L_H + ((4 * c + r) & 31) * 4);
    const v2f gm = *(LAS v2f*)(L + L_G);
    v4f acc = {0.f, 0.f, 0.f, 0.f};
    gemv_rows<0, 6>(w2v, hd, sel, acc);
    acc *= gm.y;
    lds_wait_ge(fl + 3, 8u);
    float fw[8];
    {
      float M = -INFINITY, Ls = 0.f;
      v2f st[8];
#pragma unroll
      for (int ww = 0; ww < 8; ++ww) { st[ww] = *(LAS v2f*)(L + L_ST + ww * 8); M = fmaxf(M, st[ww].x); }
#pragma unroll
      for (int ww = 0; ww < 8; ++ww) { fw[ww] = __builtin_amdgcn_exp2f(st[ww].x - M); Ls = fmaf(fw[ww], st[ww].y, Ls); }
      const float inv = gm.x / Ls;
#pragma unroll
      for (int ww = 0; ww < 8; ++ww) fw[ww] *= inv;
    }
    wait_vm<0>();
    STAMP(4);
    barrier();
    STAMP(5);
    v2f o2 = {0.f, 0.f};
#pragma unroll
    for (int ww = 0; ww < 8; ++ww) { const v2f p = *(LAS v2f*)(L + L_OP + ww * 512 + (8 * c + 2 * r) * 4); o2.x = fmaf(fw[ww], p.x, o2.x); o2.y = fmaf(fw[ww], p.y, o2.y); }
    const unsigned od = pk_bf16(o2.x, o2.y);
    gemv_rows<0, 16>(wov, od, sel, acc);
    {
      auto s0 = __builtin_amdgcn_permlane32_swap(__float_as_uint(acc.x), __float_as_uint(acc.z), false, false);
      auto s1 = __builtin_amdgcn_permlane32_swap(__float_as_uint(acc.y), __float_as_uint(acc.w), false, false);
      const float y0 = __uint_as_float(s0[0]) + dppf<XOR8>(__uint_as_float(s0[1]));
      const float y1 = __uint_as_float(s1[0]) + dppf<XOR8>(__uint_as_float(s1[1]));
      const int fo = WS_FX + ((b * 32 + hq) * 32 + h) * 128 + (8 * (c & 1) + 4 * (r & 1) + 2 * (r >> 1)) * 8;
      __builtin_amdgcn_raw_buffer_store_b128(v4u{__float_as_uint(y0), tag_f, __float_as_uint(y1), tag_f}, rws, c < 8 ? fo : OOB, 0, AUX_SC1);
    }
    STAMP(6);
    if (w == 0) {
      int io = WS_FX + (b * 32 + h) * 4096 + lane * 16;
      v4u g[4];
#pragma unroll 1
      for (unsigned spins = 0; spins < SPIN_CAP; ++spins) {
        asm volatile("" : "+v"(io)::"memory");
        bool ok = true;
#pragma unroll
        for (int i = 0; i < 4; ++i) g[i] = __builtin_amdgcn_raw_buffer_load_b128(rws, io, i * 1024, AUX_SC1);
#pragma unroll
        for (int i = 0; i < 4; ++i) ok = ok && g[i].y == tag_me && g[i].w == tag_me;
        if (__all(ok)) break;
      }
      float y0 = 0.f, y1 = 0.f;
#pragma unroll
      for (int i = 0; i < 4; ++i) { y0 += __uint_as_float(g[i].x); y1 += __uint_as_float(g[i].z); }
      y0 += dppf<XOR8>(y0); y1 += dppf<XOR8>(y1);
      y0 = rows_sum(y0); y1 = rows_sum(y1);
      if (lane < 8) *(GAS unsigned*)(out + (b * DM + 16 * h + 2 * lane) * 2) = pk_bf16(y0, y1);
      __builtin_amdgcn_raw_buffer_store_b32(tag_me, rws, lane == 0 ? WS_EP + bid * 4 : OOB, 0, AUX_SC1);
      STAMP(7);
    }
  }
  if (w16 == DIAG_W && (DIAG_BID < 0 || bid == DIAG_BID)) {
    const unsigned ta = __builtin_amdgcn_readlane(stamp, DIAG_A), tb = __builtin_amdgcn_readlane(stamp, DIAG_B);
    const unsigned iv = tb ? (tb - ta) : 0u;
    const unsigned t00 = __builtin_amdgcn_readlane(stamp, 0);
    while ((unsigned)__builtin_amdgcn_s_memrealtime() - t00 < 6000u + 10u * iv) __builtin_amdgcn_s_sleep(4);
  }
}

}

extern "C" void kernel_launch(void* const* d_in, const int* in_sizes, int n_in, void* d_out, int out_size, void* d_ws, size_t ws_size,
                              hipStream_t stream) {
  fused_kernel<<<256, 1024, 0, stream>>>((const unsigned char*)d_in[0], (const unsigned char*)d_in[1], (const unsigned char*)d_in[2],
                                         (const unsigned char*)d_in[3], (const unsigned char*)d_in[4], (const unsigned char*)d_in[6],
                                         (const unsigned char*)d_in[7], (const unsigned char*)d_in[8], (const unsigned char*)d_in[9],
                                         (unsigned char*)d_out, (unsigned char*)d_ws);
}
```

```cpp
#define STAMPS
#define KN_R 4
#define DIAG_A 0
#define DIAG_B 0
#define DIAG_W 8
#define DIAG_BID 77
#include <hip/hip_runtime.h>
#include <hip/hip_bf16.h>

#define KN_N0 20
#define KN_RW 2

namespace {

typedef unsigned v4u __attribute__((ext_vector_type(4)));
typedef unsigned v2u __attribute__((ext_vector_type(2)));
typedef float v4f __attribute__((ext_vector_type(4)));
typedef float v2f __attribute__((ext_vector_type(2)));
typedef short v4s __attribute__((ext_vector_type(4)));
typedef __bf16 v8bf __attribute__((ext_vector_type(8)));
typedef __bf16 v2bf __attribute__((ext_vector_type(2)));
#define LAS __attribute__((address_space(3)))
#define GAS __attribute__((address_space(1)))
typedef __amdgpu_buffer_rsrc_t rsrc_t;

constexpr int DM = 512, NH = 32, SK = 4096, WIN = 1024, FF = 1376;
constexpr int NCOL = 4096 + 2 * FF;
constexpr int WS_EP = 0, WS_PX = 4096, WS_FX = 512 * 1024, WS_BYTES = WS_FX + 8 * 32 * 32 * 16 * 8;
constexpr int AUX_NT = 2, AUX_SC1 = 16, OOB = 0x7ffffff0;
constexpr unsigned TAG_STEP = 0x9E3779B9u;
constexpr unsigned SPIN_CAP = 1u << 16;
constexpr int L_STG = 0;
constexpr int L_RED = 32768;
constexpr int L_OP = L_RED + 8192;
constexpr int L_Q = L_OP + 4096;
constexpr int L_H = L_Q + 256;
constexpr int L_ST = L_H + 128;
constexpr int L_G = L_ST + 64;
constexpr int L_FL = L_G + 16;
constexpr int L_BYTES = L_FL + 16;

__device__ __forceinline__ rsrc_t mk_rsrc(const void* p, int bytes) { return __builtin_amdgcn_make_buffer_rsrc((void*)p, 0, bytes, 0x00020000); }
__device__ __forceinline__ unsigned pk_bf16(float lo, float hi) { v2bf r = {(__bf16)lo, (__bf16)hi}; return __builtin_bit_cast(unsigned, r); }
__device__ __forceinline__ float rbf(float x) { return (float)(__bf16)x; }
__device__ __forceinline__ float bf_lo(unsigned u) { return __builtin_bit_cast(float, u << 16); }
__device__ __forceinline__ float bf_hi(unsigned u) { return __builtin_bit_cast(float, u & 0xffff0000u); }
__device__ __forceinline__ float dot2(unsigned a, unsigned b, float c) { return __builtin_amdgcn_fdot2_f32_bf16(__builtin_bit_cast(v2bf, a), __builtin_bit_cast(v2bf, b), c, false); }
__device__ __forceinline__ v4f mfma(v4u a, v4u b, v4f c) { return __builtin_amdgcn_mfma_f32_16x16x32_bf16(__builtin_bit_cast(v8bf, a), __builtin_bit_cast(v8bf, b), c, 0, 0, 0); }
template <int CTRL> __device__ __forceinline__ unsigned dppu(unsigned x) { return (unsigned)__builtin_amdgcn_mov_dpp((int)x, CTRL, 0xf, 0xf, true); }
template <int CTRL> __device__ __forceinline__ float dppf(float x) { return __builtin_bit_cast(float, dppu<CTRL>(__builtin_bit_cast(unsigned, x))); }
constexpr int XOR1 = 0xB1, XOR2 = 0x4E, XOR7 = 0x141, XOR8 = 0x128, BCAST = 0x150;
__device__ __forceinline__ float rows_sum(float x) {
  auto s = __builtin_amdgcn_permlane16_swap(__float_as_uint(x), __float_as_uint(x), false, false);
  x = __uint_as_float(s[0]) + __uint_as_float(s[1]);
  auto t = __builtin_amdgcn_permlane32_swap(__float_as_uint(x), __float_as_uint(x), false, false);
  return __uint_as_float(t[0]) + __uint_as_float(t[1]);
}
__device__ __forceinline__ float rows_max(float x) {
  auto s = __builtin_amdgcn_permlane16_swap(__float_as_uint(x), __float_as_uint(x), false, false);
  x = fmaxf(__uint_as_float(s[0]), __uint_as_float(s[1]));
  auto t = __builtin_amdgcn_permlane32_swap(__float_as_uint(x), __float_as_uint(x), false, false);
  return fmaxf(__uint_as_float(t[0]), __uint_as_float(t[1]));
}
__device__ __forceinline__ float wave_sum(float x) { x += dppf<XOR8>(x); x += dppf<XOR7>(x); x += dppf<XOR2>(x); x += dppf<XOR1>(x); return rows_sum(x); }
__device__ __forceinline__ float wave_max(float x) { x = fmaxf(x, dppf<XOR8>(x)); x = fmaxf(x, dppf<XOR7>(x)); x = fmaxf(x, dppf<XOR2>(x)); x = fmaxf(x, dppf<XOR1>(x)); return rows_max(x); }
template <int N> __device__ __forceinline__ void wait_vm() { asm volatile("s_waitcnt vmcnt(%0)" ::"i"(N) : "memory"); }
__device__ __forceinline__ void wait_lds() { asm volatile("s_waitcnt lgkmcnt(0)" ::: "memory"); }
__device__ __forceinline__ void barrier() { asm volatile("s_waitcnt lgkmcnt(0)\n\ts_barrier" ::: "memory"); }
__device__ __forceinline__ void lds_wait_ge(LAS unsigned* p, unsigned want) {
  while ((unsigned)__builtin_amdgcn_readfirstlane(__hip_atomic_load(p, __ATOMIC_RELAXED, __HIP_MEMORY_SCOPE_WORKGROUP)) < want) __builtin_amdgcn_s_sleep(2);
  asm volatile("" ::: "memory");
}
__device__ __forceinline__ void lds_signal(LAS unsigned* p, int lane) {
  wait_lds();
  if (lane == 0) __hip_atomic_fetch_add(p, 1u, __ATOMIC_RELAXED, __HIP_MEMORY_SCOPE_WORKGROUP);
  asm volatile("" ::: "memory");
}
#define SB() __builtin_amdgcn_sched_barrier(0)
constexpr int rev4(int i) { return ((i & 1) << 3) | ((i & 2) << 1) | ((i & 4) >> 1) | ((i & 8) >> 3); }
__device__ __forceinline__ int wg_of(int b, int h) { return (h >> 2) + 8 * ((h & 3) + 4 * b); }

#define STAMP(i) do { const unsigned t_ = (unsigned)__builtin_amdgcn_s_memrealtime(); stamp = (lane == (i)) ? t_ : stamp; } while (0)

struct Stream {
  rsrc_t rk, rv;
  int voff, lane;
  LAS unsigned char* L;
  v4u a[64];
  v4u q;
  float lv[5];
  unsigned pp[2], msk[4];
  float s[2];
  v4f acc;
  int w;
  unsigned stamp;
};
constexpr int k_done(int n) { int d = n < KN_N0 ? 0 : 2 * (n - KN_N0 + 1); int lim = n - KN_R + 1; d = d < lim ? d : lim; return d < 0 ? 0 : (d > 32 ? 32 : d); }
constexpr int soft_step() { int n = 0; while (k_done(n) < 32) ++n; return n + 1; }
constexpr int v_done(int n) { int d = n <= soft_step() ? 0 : 2 * (n - soft_step()); int lim = n - KN_R + 1 - 32; d = d < lim ? d : lim; return d < 0 ? 0 : (d > 32 ? 32 : d); }

template <int I> __device__ __forceinline__ void eat_k(Stream& S) {
  float d = dot2(S.a[I].x, S.q.x, 0.f);
  d = dot2(S.a[I].y, S.q.y, d); d = dot2(S.a[I].z, S.q.z, d); d = dot2(S.a[I].w, S.q.w, d);
  const int c = S.lane;
#define TR_LEVEL(LV, CTRL, BIT) \
  if constexpr ((I & (1 << LV)) == 0) { S.lv[LV] = d; return; } \
  { const float t1 = S.lv[LV] + dppf<CTRL>(S.lv[LV]), t2 = d + dppf<CTRL>(d); d = (c & BIT) ? t2 : t1; }
  TR_LEVEL(0, XOR8, 8) TR_LEVEL(1, XOR7, 4) TR_LEVEL(2, XOR2, 2) TR_LEVEL(3, XOR1, 1)
#undef TR_LEVEL
  S.s[I >> 4] = d;
}
template <int I> __device__ __forceinline__ void eat_v(Stream& S) {
  const unsigned t = dppu<BCAST + rev4(I & 15)>(S.pp[I >> 4]);
  const v4u cf = {t & S.msk[0], t & S.msk[1], t & S.msk[2], t & S.msk[3]};
  S.acc = mfma(cf, S.a[32 + I], S.acc);
}
template <int LO, int HI> __device__ __forceinline__ void eat_ks(Stream& S) { if constexpr (LO < HI) { eat_k<LO>(S); eat_ks<LO + 1, HI>(S); } }
template <int LO, int HI> __device__ __forceinline__ void eat_vs(Stream& S) { if constexpr (LO < HI) { eat_v<LO>(S); SB(); eat_vs<LO + 1, HI>(S); } }

template <int N> __device__ __forceinline__ void stream_step(Stream& S) {
  if constexpr (N < 64) {
    unsigned& stamp = S.stamp; const int lane = S.lane;
    if constexpr (N >= KN_R) wait_vm<KN_R - 1>();
    if constexpr (N < 32) S.a[N] = __builtin_amdgcn_raw_buffer_load_b128(S.rk, S.voff, N * 8192, AUX_NT);
    else S.a[N] = __builtin_amdgcn_raw_buffer_load_b128(S.rv, S.voff, (N - 32) * 8192, AUX_NT);
    SB();
    if constexpr (N == KN_R - 1) barrier();
    if constexpr (N == KN_R) STAMP(1);
    if constexpr (N == KN_N0) {
      STAMP(2);
      lds_wait_ge((LAS unsigned*)(S.L + L_FL) + 1, 1u);
      S.q = *(LAS v4u*)(S.L + L_Q + (S.lane & 15) * 16);
      STAMP(3);
    }
    eat_ks<(N > 0 ? k_done(N - 1) : 0), k_done(N)>(S);
    if constexpr (N == soft_step()) {
      constexpr float C2 = 0.08838834764831845f * 1.4426950408889634f;
      const float s0 = S.s[0] * C2, s1 = S.s[1] * C2;
      const float m = wave_max(fmaxf(s0, s1));
      const float p0 = __builtin_amdgcn_exp2f(s0 - m), p1 = __builtin_amdgcn_exp2f(s1 - m);
      S.pp[0] = pk_bf16(p0, p0); S.pp[1] = pk_bf16(p1, p1);
      const float l = wave_sum(bf_lo(S.pp[0]) + bf_lo(S.pp[1]));
      if (S.lane == 0) *(LAS v2f*)(S.L + L_ST + S.w * 8) = v2f{m, l};
      lds_signal((LAS unsigned*)(S.L + L_FL) + 3, S.lane);
      STAMP(4);
    }
    eat_vs<(N > 0 ? v_done(N - 1) : 0), v_done(N)>(S);
    SB();
    stream_step<N + 1>(S);
  }
}

template <int M, int MN> __device__ __forceinline__ void gemv_rows(const v4u* wv, unsigned od, const unsigned* sel, v4f& acc) {
  if constexpr (M < MN) {
    const unsigned t = dppu<BCAST + M>(od);
    const v4u cf = {__builtin_amdgcn_perm(t, t, sel[0]), __builtin_amdgcn_perm(t, t, sel[1]), __builtin_amdgcn_perm(t, t, sel[2]), __builtin_amdgcn_perm(t, t, sel[3])};
    acc = mfma(cf, wv[M], acc);
    gemv_rows<M + 1, MN>(wv, od, sel, acc);
  }
}
template <int N> __device__ __forceinline__ void weight_loads(v4u* w2v, v4u* wov, rsrc_t r2, rsrc_t ro, int v2, int vo, int s5) {
  if constexpr (N < 22) {
    if constexpr (N >= KN_RW) wait_vm<KN_RW - 1>();
    if constexpr (N < 5) w2v[N] = __builtin_amdgcn_raw_buffer_load_b128(r2, v2, N * 8192, AUX_NT);
    else if constexpr (N == 5) w2v[N] = __builtin_amdgcn_raw_buffer_load_b128(r2, v2, s5, AUX_NT);
    else wov[N - 6] = __builtin_amdgcn_raw_buffer_load_b128(ro, vo, (N - 6) * 8192, AUX_NT);
    SB();
    weight_loads<N + 1>(w2v, wov, r2, ro, v2, vo, s5);
  }
}

__global__ void __launch_bounds__(1024, 4)
fused_kernel(const unsigned char* __restrict__ x, const unsigned char* __restrict__ kc, const unsigned char* __restrict__ vc,
             const unsigned char* __restrict__ wq, const unsigned char* __restrict__ wo, const unsigned char* __restrict__ w1,
             const unsigned char* __restrict__ w3, const unsigned char* __restrict__ w2, const unsigned char* __restrict__ gw,
             unsigned char* out, unsigned char* ws) {
  __shared__ __attribute__((aligned(16))) unsigned char smem[L_BYTES];
  LAS unsigned char* L = (LAS unsigned char*)smem;
  const int tid = threadIdx.x, lane = tid & 63, w16 = __builtin_amdgcn_readfirstlane(tid >> 6), w = w16 & 7;
  const int bid = blockIdx.x, xcd = bid & 7, j = bid >> 3, h = 4 * xcd + (j & 3), b = j >> 2;
  const int r = lane >> 4, c = lane & 15;
  unsigned stamp = 0;
  STAMP(0);
  if (w16 >= 8) {
    Stream S;
    const size_t base = ((size_t)(b * NH + h) * SK + (SK - WIN)) * 256;
    S.rk = mk_rsrc(kc + base, WIN * 256); S.rv = mk_rsrc(vc + base, WIN * 256);
    S.voff = lane * 16 + w * 1024; S.lane = lane; S.L = L; S.w = w;
    S.acc = v4f{0.f, 0.f, 0.f, 0.f};
#pragma unroll
    for (int d = 0; d < 4; ++d) S.msk[d] = (c >> 1) == d ? ((c & 1) ? 0xffff0000u : 0x0000ffffu) : 0u;
    S.stamp = stamp;
    stream_step<0>(S);
    eat_vs<v_done(63), 32>(S);
    stamp = S.stamp;
    STAMP(5);
    if (lane < 32) *(LAS v4f*)(L + L_OP + w * 512 + (8 * c + 4 * r) * 4) = S.acc;
    barrier();
  } else {
    const rsrc_t rws = mk_rsrc(ws, WS_BYTES);
    const int t0 = xcd * 86 / 8, nt = (xcd + 1) * 86 / 8 - t0, t = t0 + j - 16;
    const bool isq = j < 16, valid = j < 16 + nt;
    const unsigned char* ub = isq ? wq : ((t & 1) ? w3 : w1);
    const int ustr = isq ? 8192 : 2 * FF, ucb = isq ? (16 * xcd + j) * 64 : (t >> 1) * 64;
    const int ucol = isq ? (16 * xcd + j) * 32 : 4096 + (t & 1) * FF + (t >> 1) * 32;
    const rsrc_t ru = mk_rsrc(ub, valid ? DM * ustr : 0);
    const int vu = (64 * w + (lane >> 2)) * ustr + ucb + (lane & 3) * 16;
    v4u u[4];
#pragma unroll
    for (int i = 0; i < 4; ++i) u[i] = __builtin_amdgcn_raw_buffer_load_b128(ru, vu, i * 16 * ustr, AUX_NT);
    const rsrc_t rx = mk_rsrc(x, 8 * DM * 2);
    const v4u xa0 = __builtin_amdgcn_raw_buffer_load_b128(rx, ((c & 7) * DM + 64 * w + 8 * r) * 2, 0, AUX_NT);
    const v4u xa1 = __builtin_amdgcn_raw_buffer_load_b128(rx, ((c & 7) * DM + 64 * w + 8 * r) * 2, 64, AUX_NT);
    const int fch = (t >> 1) * 4 + ((lane & 31) >> 3);
    const int hcons = isq ? 4 * xcd + (j >> 2) : (8 * fch + 7) / 43;
    const unsigned tag_u = __builtin_amdgcn_raw_buffer_load_b32(rws, valid ? WS_EP + wg_of(w, hcons) * 4 : OOB, 0, AUX_SC1) + TAG_STEP;
    const int hq = 4 * w + ((c & 7) >> 1);
    const unsigned tag_f = __builtin_amdgcn_raw_buffer_load_b32(rws, WS_EP + wg_of(b, hq) * 4, 0, AUX_SC1) + TAG_STEP;
    const unsigned tag_me = __builtin_amdgcn_raw_buffer_load_b32(rws, WS_EP + bid * 4, 0, AUX_SC1) + TAG_STEP;
    v4u gx = {0, 0, 0, 0}, gg = {0, 0, 0, 0};
    if (w == 1) {
      gx = __builtin_amdgcn_raw_buffer_load_b128(rx, b * DM * 2 + lane * 16, 0, AUX_NT);
      gg = __builtin_amdgcn_raw_buffer_load_b128(mk_rsrc(gw, DM * 2), lane * 16, 0, AUX_NT);
    }
    if (tid < 4) ((LAS unsigned*)(L + L_FL))[tid] = 0u;
    barrier();
    LAS unsigned* fl = (LAS unsigned*)(L + L_FL);
    if (valid) {
      LAS unsigned char* stg = L + L_STG + w * 4096;
#pragma unroll
      for (int i = 0; i < 4; ++i) *(LAS v4u*)(stg + i * 1024 + lane * 16) = u[i];
      STAMP(1);
      LAS unsigned char* tr = stg + (8 * r + (c >> 2)) * 64 + (c & 3) * 8;
      v4f ua[2];
#pragma unroll
      for (int tt = 0; tt < 2; ++tt) {
        ua[tt] = v4f{0.f, 0.f, 0.f, 0.f};
#pragma unroll
        for (int s = 0; s < 2; ++s) {
          const v4s lo = __builtin_amdgcn_ds_read_tr16_b64_v4i16((LAS v4s*)(tr + s * 2048 + tt * 32));
          const v4s hi = __builtin_amdgcn_ds_read_tr16_b64_v4i16((LAS v4s*)(tr + s * 2048 + tt * 32 + 256));
          const v2u l2 = __builtin_bit_cast(v2u, lo), h2 = __builtin_bit_cast(v2u, hi);
          ua[tt] = mfma(s ? xa1 : xa0, v4u{l2.x, l2.y, h2.x, h2.y}, ua[tt]);
        }
      }
      if (lane < 32) {
        *(LAS v4f*)(L + L_RED + w * 1024 + lane * 16) = ua[0];
        *(LAS v4f*)(L + L_RED + w * 1024 + 512 + lane * 16) = ua[1];
      }
      lds_signal(fl + 0, lane);
      lds_wait_ge(fl + 0, 8u);
      const int ro_ = L_RED + ((lane >> 4) & 1) * 512 + ((w >> 2) * 16 + c) * 16 + (w & 3) * 4;
      float sum = 0.f;
#pragma unroll
      for (int ww = 0; ww < 8; ++ww) sum += *(LAS float*)(L + ro_ + ww * 1024);
      __builtin_amdgcn_raw_buffer_store_b64(v2u{__float_as_uint(sum), tag_u}, rws, lane < 32 ? WS_PX + (w * NCOL + ucol + lane) * 8 : OOB, 0, AUX_SC1);
      STAMP(2);
    }
    const int c0 = h * 43 / 8, nch = (h + 1) * 43 / 8 - c0;
    if (w == 0) {
      v4u g;
      int po = WS_PX + (b * NCOL + h * 128 + 2 * lane) * 8;
#pragma unroll 1
      for (unsigned spins = 0; spins < SPIN_CAP; ++spins) {
        asm volatile("" : "+v"(po)::"memory");
        g = __builtin_amdgcn_raw_buffer_load_b128(rws, po, 0, AUX_SC1);
        if (__all(g.y == tag_me && g.w == tag_me)) break;
      }
      *(LAS unsigned*)(L + L_Q + lane * 4) = pk_bf16(__uint_as_float(g.x), __uint_as_float(g.z));
      lds_signal(fl + 1, lane);
      STAMP(3);
    }
    if (w == 1) {
      float s = 0.f;
#pragma unroll
      for (int d = 0; d < 4; ++d) s += rbf(bf_lo(gx[d]) * bf_lo(gg[d])) + rbf(bf_hi(gx[d]) * bf_hi(gg[d]));
      s = rbf(wave_sum(s));
      const float g = rbf(1.0f / (1.0f + __expf(-s)));
      if (lane == 0) *(LAS v2f*)(L + L_G) = v2f{g, rbf(1.0f - g)};
      const bool on = lane < 8 * nch;
      int po = on ? WS_PX + (b * NCOL + 4096 + 8 * c0 + lane) * 8 : OOB;
      v2u ga, up;
#pragma unroll 1
      for (unsigned spins = 0; spins < SPIN_CAP; ++spins) {
        asm volatile("" : "+v"(po)::"memory");
        ga = __builtin_amdgcn_raw_buffer_load_b64(rws, po, 0, AUX_SC1);
        up = __builtin_amdgcn_raw_buffer_load_b64(rws, po, FF * 8, AUX_SC1);
        if (__all(!on || (ga.y == tag_me && up.y == tag_me))) break;
      }
      const float gv = rbf(__uint_as_float(ga.x)), uv = rbf(__uint_as_float(up.x));
      const float hv = on ? rbf(gv / (1.0f + __expf(-gv))) * uv : 0.f;
      *(LAS __bf16*)(L + L_H + lane * 2) = (__bf16)hv;
      lds_signal(fl + 2, lane);
      STAMP(3);
    }
    v4u w2v[6], wov[16];
    const int vrow = (2 * r + (c >> 3)) * 1024 + w * 128 + (c & 7) * 16;
    weight_loads<0>(w2v, wov, mk_rsrc(w2, FF * 1024), mk_rsrc(wo, 4096 * 1024), c0 * 8192 + vrow, h * 131072 + vrow, nch == 6 ? 5 * 8192 : OOB);
    unsigned sel[4];
#pragma unroll
    for (int d = 0; d < 4; ++d) {
      const unsigned pick = (c & 8) ? 0x0302u : 0x0100u;
      sel[d] = ((c & 7) >> 1) == d ? ((c & 1) ? (pick << 16) | 0x0c0cu : 0x0c0c0000u | pick) : 0x0c0c0c0cu;
    }
    lds_wait_ge(fl + 2, 1u);
    const unsigned hd = *(LAS unsigned*)(L + L_H + ((4 * c + r) & 31) * 4);
    const v2f gm = *(LAS v2f*)(L + L_G);
    v4f acc = {0.f, 0.f, 0.f, 0.f};
    gemv_rows<0, 6>(w2v, hd, sel, acc);
    acc *= gm.y;
    lds_wait_ge(fl + 3, 8u);
    float fw[8];
    {
      float M = -INFINITY, Ls = 0.f;
      v2f st[8];
#pragma unroll
      for (int ww = 0; ww < 8; ++ww) { st[ww] = *(LAS v2f*)(L + L_ST + ww * 8); M = fmaxf(M, st[ww].x); }
#pragma unroll
      for (int ww = 0; ww < 8; ++ww) { fw[ww] = __builtin_amdgcn_exp2f(st[ww].x - M); Ls = fmaf(fw[ww], st[ww].y, Ls); }
      const float inv = gm.x / Ls;
#pragma unroll
      for (int ww = 0; ww < 8; ++ww) fw[ww] *= inv;
    }
    wait_vm<0>();
    STAMP(4);
    barrier();
    STAMP(5);
    v2f o2 = {0.f, 0.f};
#pragma unroll
    for (int ww = 0; ww < 8; ++ww) { const v2f p = *(LAS v2f*)(L + L_OP + ww * 512 + (8 * c + 2 * r) * 4); o2.x = fmaf(fw[ww], p.x, o2.x); o2.y = fmaf(fw[ww], p.y, o2.y); }
    const unsigned od = pk_bf16(o2.x, o2.y);
    gemv_rows<0, 16>(wov, od, sel, acc);
    {
      auto s0 = __builtin_amdgcn_permlane32_swap(__float_as_uint(acc.x), __float_as_uint(acc.z), false, false);
      auto s1 = __builtin_amdgcn_permlane32_swap(__float_as_uint(acc.y), __float_as_uint(acc.w), false, false);
      const float y0 = __uint_as_float(s0[0]) + dppf<XOR8>(__uint_as_float(s0[1]));
      const float y1 = __uint_as_float(s1[0]) + dppf<XOR8>(__uint_as_float(s1[1]));
      const int fo = WS_FX + ((b * 32 + hq) * 32 + h) * 128 + (8 * (c & 1) + 4 * (r & 1) + 2 * (r >> 1)) * 8;
      __builtin_amdgcn_raw_buffer_store_b128(v4u{__float_as_uint(y0), tag_f, __float_as_uint(y1), tag_f}, rws, c < 8 ? fo : OOB, 0, AUX_SC1);
    }
    STAMP(6);
    if (w == 0) {
      int io = WS_FX + (b * 32 + h) * 4096 + lane * 16;
      v4u g[4];
#pragma unroll 1
      for (unsigned spins = 0; spins < SPIN_CAP; ++spins) {
        asm volatile("" : "+v"(io)::"memory");
        bool ok = true;
#pragma unroll
        for (int i = 0; i < 4; ++i) g[i] = __builtin_amdgcn_raw_buffer_load_b128(rws, io, i * 1024, AUX_SC1);
#pragma unroll
        for (int i = 0; i < 4; ++i) ok = ok && g[i].y == tag_me && g[i].w == tag_me;
        if (__all(ok)) break;
      }
      float y0 = 0.f, y1 = 0.f;
#pragma unroll
      for (int i = 0; i < 4; ++i) { y0 += __uint_as_float(g[i].x); y1 += __uint_as_float(g[i].z); }
      y0 += dppf<XOR8>(y0); y1 += dppf<XOR8>(y1);
      y0 = rows_sum(y0); y1 = rows_sum(y1);
      if (lane < 8) *(GAS unsigned*)(out + (b * DM + 16 * h + 2 * lane) * 2) = pk_bf16(y0, y1);
      __builtin_amdgcn_raw_buffer_store_b32(tag_me, rws, lane == 0 ? WS_EP + bid * 4 : OOB, 0, AUX_SC1);
      STAMP(7);
    }
  }
}

__global__ void k_mark(unsigned char* ws) { if (threadIdx.x == 0) __hip_atomic_store((GAS unsigned*)(ws + (8 << 20)), (unsigned)__builtin_amdgcn_s_memrealtime(), __ATOMIC_RELAXED, __HIP_MEMORY_SCOPE_AGENT); }
__global__ void k_wait(unsigned char* ws) { const unsigned t00 = __builtin_amdgcn_readfirstlane(__hip_atomic_load((GAS unsigned*)(ws + (8 << 20)), __ATOMIC_RELAXED, __HIP_MEMORY_SCOPE_AGENT));
  while ((unsigned)__builtin_amdgcn_s_memrealtime() - t00 < 6000u) __builtin_amdgcn_s_sleep(4); }
}

extern "C" void kernel_launch(void* const* d_in, const int* in_sizes, int n_in, void* d_out, int out_size, void* d_ws, size_t ws_size,
                              hipStream_t stream) {
  k_mark<<<1, 64, 0, stream>>>((unsigned char*)d_ws);
  fused_kernel<<<256, 1024, 0, stream>>>((const unsigned char*)d_in[0], (const unsigned char*)d_in[1], (const unsigned char*)d_in[2],
                                         (const unsigned char*)d_in[3], (const unsigned char*)d_in[4], (const unsigned char*)d_in[6],
                                         (const unsigned char*)d_in[7], (const unsigned char*)d_in[8], (const unsigned char*)d_in[9],
                                         (unsigned char*)d_out, (unsigned char*)d_ws);
  k_wait<<<1, 64, 0, stream>>>((unsigned char*)d_ws);
}
```

```cpp
#include <hip/hip_runtime.h>
#include <hip/hip_bf16.h>

#define KN_R 3
#define KN_RHI 5
#define KN_RLO 2
#define KN_TLO 30
#define KN_THI 50
#define KN_NP 20
#define KN_DR 7
#define KN_TA 20
#define KN_DT 496
#define KN_DT2 472
#define KN_NB 44
#define KN_N0 20
#define KN_NF 52
#define KN_NQ 3
#define KN_NH 8
#define KN_PG 1
#define KN_WFAST 1
#define KN_AK 1
#define KN_PD 16

namespace {

typedef unsigned v4u __attribute__((ext_vector_type(4)));
typedef unsigned v2u __attribute__((ext_vector_type(2)));
typedef unsigned v8u __attribute__((ext_vector_type(8)));
typedef __bf16 v16bf __attribute__((ext_vector_type(16)));
typedef float v4f __attribute__((ext_vector_type(4)));
typedef float v2f __attribute__((ext_vector_type(2)));
typedef short v4s __attribute__((ext_vector_type(4)));
typedef __bf16 v8bf __attribute__((ext_vector_type(8)));
typedef __bf16 v2bf __attribute__((ext_vector_type(2)));
typedef unsigned long long v8ul __attribute__((ext_vector_type(8)));
typedef unsigned long long v2ul __attribute__((ext_vector_type(2)));
#define LAS __attribute__((address_space(3)))
#define GAS __attribute__((address_space(1)))
typedef __amdgpu_buffer_rsrc_t rsrc_t;

constexpr int DM = 512, NH = 32, SK = 4096, WIN = 1024, FF = 1376;
constexpr int NCOL = 4096 + 2 * FF;
constexpr int WS_EP = 0, WS_PX = 4096, PX_HALF = 8 * NCOL * 4, WS_TP = 480 * 1024, WS_AK = 488 * 1024, WS_FX = 512 * 1024, WS_BYTES = WS_FX + 8 * 32 * 32 * 16 * 4;
constexpr int AUX_NT = 2, AUX_SC1 = 16, OOB = 0x40000000;
constexpr unsigned TAG_STEP = 0x01010101u;
constexpr unsigned SPIN_CAP = 1u << 16;
constexpr int R0 = 2;
constexpr int L_STG = 0;
constexpr int L_K0 = 65536;
constexpr int L_RED = L_K0 + 8 * R0 * 1024;
constexpr int L_X = L_RED + 16384;
constexpr int OPP = 320;
constexpr int L_OP = L_X + 8192;
constexpr int L_EP = L_OP + 8 * OPP;
constexpr int L_Q = L_EP + 1024;
constexpr int L_H = L_Q + 256;
constexpr int L_ST = L_H + 128;
constexpr int L_G = L_ST + 64;
constexpr int L_FL = L_G + 16;
constexpr int L_BYTES = L_FL + 32;

__device__ __forceinline__ rsrc_t mk_rsrc(const void* p, int bytes) { return __builtin_amdgcn_make_buffer_rsrc((void*)p, 0, bytes, 0x00020000); }
__device__ __forceinline__ v4u mk_desc(unsigned long long a, int bytes) { return v4u{(unsigned)a, (unsigned)(a >> 32) & 0xffffu, (unsigned)bytes, 0x00020000u}; }
__device__ __forceinline__ unsigned pk_bf16(float lo, float hi) { v2bf r = {(__bf16)lo, (__bf16)hi}; return __builtin_bit_cast(unsigned, r); }
__device__ __forceinline__ float rbf(float x) { return (float)(__bf16)x; }
__device__ __forceinline__ float bf_lo(unsigned u) { return __builtin_bit_cast(float, u << 16); }
__device__ __forceinline__ float bf_hi(unsigned u) { return __builtin_bit_cast(float, u & 0xffff0000u); }
__device__ __forceinline__ float dot2(unsigned a, unsigned b, float c) { return __builtin_amdgcn_fdot2_f32_bf16(__builtin_bit_cast(v2bf, a), __builtin_bit_cast(v2bf, b), c, false); }
__device__ __forceinline__ v4f mfma(v4u a, v4u b, v4f c) { return __builtin_amdgcn_mfma_f32_16x16x32_bf16(__builtin_bit_cast(v8bf, a), __builtin_bit_cast(v8bf, b), c, 0, 0, 0); }
__device__ __forceinline__ v4f smfma(v4u a, v8u b, v4f c, int idx) { return __builtin_amdgcn_smfmac_f32_16x16x64_bf16(__builtin_bit_cast(v8bf, a), __builtin_bit_cast(v16bf, b), c, idx, 0, 0); }
template <int CTRL> __device__ __forceinline__ unsigned dppu(unsigned x) { return (unsigned)__builtin_amdgcn_mov_dpp((int)x, CTRL, 0xf, 0xf, true); }
template <int CTRL> __device__ __forceinline__ float dppf(float x) { return __builtin_bit_cast(float, dppu<CTRL>(__builtin_bit_cast(unsigned, x))); }
constexpr int XOR1 = 0xB1, XOR2 = 0x4E, XOR7 = 0x141, XOR8 = 0x128, ROR4 = 0x124, BCAST = 0x150;
__device__ __forceinline__ float swap32_sum(float x) {
  auto t = __builtin_amdgcn_permlane32_swap(__float_as_uint(x), __float_as_uint(x), false, false);
  return __uint_as_float(t[0]) + __uint_as_float(t[1]);
}
__device__ __forceinline__ float rows_sum(float x) {
  auto s = __builtin_amdgcn_permlane16_swap(__float_as_uint(x), __float_as_uint(x), false, false);
  return swap32_sum(__uint_as_float(s[0]) + __uint_as_float(s[1]));
}
__device__ __forceinline__ float rows_max(float x) {
  auto s = __builtin_amdgcn_permlane16_swap(__float_as_uint(x), __float_as_uint(x), false, false);
  x = fmaxf(__uint_as_float(s[0]), __uint_as_float(s[1]));
  auto t = __builtin_amdgcn_permlane32_swap(__float_as_uint(x), __float_as_uint(x), false, false);
  return fmaxf(__uint_as_float(t[0]), __uint_as_float(t[1]));
}
__device__ __forceinline__ float wave_sum(float x) { x += dppf<XOR8>(x); x += dppf<XOR7>(x); x += dppf<XOR2>(x); x += dppf<XOR1>(x); return rows_sum(x); }
__device__ __forceinline__ float wave_max(float x) { x = fmaxf(x, dppf<XOR8>(x)); x = fmaxf(x, dppf<XOR7>(x)); x = fmaxf(x, dppf<XOR2>(x)); x = fmaxf(x, dppf<XOR1>(x)); return rows_max(x); }
template <int N> __device__ __forceinline__ void wait_vm() { asm volatile("s_waitcnt vmcnt(%0)" ::"i"(N) : "memory"); }
__device__ __forceinline__ void barrier() { asm volatile("s_waitcnt lgkmcnt(0)\n\ts_barrier" ::: "memory"); }
__device__ __forceinline__ unsigned lds_peek(LAS unsigned* p) { return __builtin_amdgcn_readfirstlane(__hip_atomic_load(p, __ATOMIC_RELAXED, __HIP_MEMORY_SCOPE_WORKGROUP)); }
__device__ __forceinline__ void lds_wait_ge(LAS unsigned* p, unsigned want) {
  while (lds_peek(p) < want) __builtin_amdgcn_s_sleep(2);
  asm volatile("" ::: "memory");
}
__device__ __forceinline__ void lds_signal(LAS unsigned* p, int lane) {
  asm volatile("s_waitcnt lgkmcnt(0)" ::: "memory");
  if (lane == 0) __hip_atomic_fetch_add(p, 1u, __ATOMIC_RELAXED, __HIP_MEMORY_SCOPE_WORKGROUP);
  asm volatile("" ::: "memory");
}
__device__ __forceinline__ v4u tag_bytes(unsigned tag) { return v4u{tag & 0xffu, (tag >> 8) & 0xffu, (tag >> 16) & 0xffu, tag >> 24}; }
__device__ __forceinline__ v4u tag4(float y, v4u tb) {
  const unsigned u = (__float_as_uint(y) + 0x80u) & 0xffffff00u;
  return v4u{u | tb.x, dppu<0x55>(u) | tb.y, dppu<0xAA>(u) | tb.z, dppu<0xFF>(u) | tb.w};
}
__device__ __forceinline__ bool has_tag(v4u g, unsigned tag) {
  return __builtin_amdgcn_perm(__builtin_amdgcn_perm(g.w, g.z, 0x0c0c0400u), __builtin_amdgcn_perm(g.y, g.x, 0x0c0c0400u), 0x05040100u) == tag;
}
__device__ __forceinline__ v4f untag(v4u g) { return __builtin_bit_cast(v4f, g & 0xffffff00u); }
#define PAGE() asm volatile("s_branch 1f\n\t.p2align 12\n1:" ::: "memory")
#define SB() __builtin_amdgcn_sched_barrier(0)
#define PIN_S(x) asm volatile("" : "+s"(x))
constexpr int rev4(int i) { return ((i & 1) << 3) | ((i & 2) << 1) | ((i & 4) >> 1) | ((i & 8) >> 3); }

#define STAMP(i) do {} while (0)
#define STAMP_OUT() do {} while (0)

struct Stream {
  rsrc_t rk, rv, rws;
  int voff, lane, w, tpo, lag;
  LAS unsigned char* L;
  v4u a[64], q, tp;
  float lv[4], s[2], m, fac;
  unsigned pp[2], msk[4], tag, tp0;
  v4f acc;
};
constexpr int k_done(int n) { int d = n < KN_N0 ? 0 : 2 * (n - KN_N0 + 1); int lim = n - KN_RHI + 1; d = d < lim ? d : lim; return d < 0 ? 0 : (d > 32 ? 32 : d); }
constexpr int soft_step() { int n = 0; while (k_done(n) < 32) ++n; return n + 1; }
constexpr int v_done(int n) { int d = n <= soft_step() ? 0 : 2 * (n - soft_step()); int lim = n - KN_RHI + 1 - 32; d = d < lim ? d : lim; return d < 0 ? 0 : (d > 32 ? 32 : d); }
constexpr int sched(int n) { return ((n < KN_NB ? n : KN_NB) * KN_DT + (n < KN_NB ? 0 : n - KN_NB) * KN_DT2) / 16; }
constexpr int NC = KN_NP + KN_DR + KN_RHI + 1;

template <int I> __device__ __forceinline__ void eat_k(Stream& S) {
  if constexpr (I < R0) S.a[I] = *(LAS v4u*)(S.L + L_K0 + (S.w * R0 + I) * 1024 + S.lane * 16);
  float d = dot2(S.a[I].x, S.q.x, 0.f);
  d = dot2(S.a[I].y, S.q.y, d); d = dot2(S.a[I].z, S.q.z, d); d = dot2(S.a[I].w, S.q.w, d);
  const int c = S.lane;
#define TR_LEVEL(LV, CTRL, BIT) \
  if constexpr ((I & (1 << LV)) == 0) { S.lv[LV] = d; return; } \
  { const float t1 = S.lv[LV] + dppf<CTRL>(S.lv[LV]), t2 = d + dppf<CTRL>(d); d = (c & BIT) ? t2 : t1; }
  TR_LEVEL(0, XOR8, 8) TR_LEVEL(1, XOR7, 4) TR_LEVEL(2, XOR2, 2) TR_LEVEL(3, XOR1, 1)
#undef TR_LEVEL
  S.s[I >> 4] = d;
}
template <int I> __device__ __forceinline__ void eat_v(Stream& S) {
  const unsigned t = dppu<BCAST + rev4(I & 15)>(S.pp[I >> 4]);
  const v4u cf = {t & S.msk[0], t & S.msk[1], t & S.msk[2], t & S.msk[3]};
  S.acc = mfma(cf, S.a[32 + I], S.acc);
}
template <int LO, int HI> __device__ __forceinline__ void eat_ks(Stream& S) { if constexpr (LO < HI) { eat_k<LO>(S); eat_ks<LO + 1, HI>(S); } }
template <int LO, int HI> __device__ __forceinline__ void eat_vs(Stream& S) { if constexpr (LO < HI) { eat_v<LO>(S); SB(); eat_vs<LO + 1, HI>(S); } }

template <int N> __device__ __forceinline__ void stream_step(Stream& S) {
  if constexpr (N < 64) {
    LAS unsigned* fl = (LAS unsigned*)(S.L + L_FL);
    if constexpr (KN_RHI > KN_R && N >= NC) {
      if constexpr ((N - NC) % 2 == 0) {
        const unsigned org = lds_peek(fl + 4);
        const int lag = (int)((unsigned)__builtin_amdgcn_s_memrealtime() - org) - (KN_TA + sched(N));
        S.lag = org ? lag : -1;
      }
      asm volatile("s_cmpk_gt_i32 %0, %1\n\ts_cbranch_scc1 1f\n\ts_cmpk_gt_i32 %0, 0\n\ts_cbranch_scc1 2f\n\ts_cmpk_gt_i32 %0, %2\n\ts_cbranch_scc1 3f\n\t"
                   "s_waitcnt vmcnt(%3)\n3:\n\ts_waitcnt vmcnt(%4)\n2:\n\ts_waitcnt vmcnt(%5)\n1:\n\ts_waitcnt vmcnt(%6)"
                   ::"s"(S.lag), "i"(KN_THI), "i"(-KN_TLO & 0xffff), "i"(KN_RLO - 1), "i"(KN_R - 1), "i"(KN_R), "i"(KN_RHI - 1) : "memory", "scc");
    } else if constexpr (N >= KN_R) wait_vm<KN_R - 1>();
    if constexpr (N < 32) S.a[N] = __builtin_amdgcn_raw_buffer_load_b128(S.rk, S.voff, N * 8192, AUX_NT);
    else S.a[N] = __builtin_amdgcn_raw_buffer_load_b128(S.rv, S.voff, (N - 32) * 8192, AUX_NT);
    SB();
    if constexpr (N == 4) STAMP(1);
    if constexpr (N == 8) STAMP(11);
    if constexpr (N == 14) STAMP(12);
    if constexpr (N == 50) STAMP(13);
    if constexpr (KN_RHI > KN_R) {
      if constexpr (N == KN_NP) {
        if (S.w == 0) {
          S.tp0 = (unsigned)__builtin_amdgcn_s_memrealtime();
          lds_wait_ge(fl + 0, 8u);
          S.tag = *(LAS unsigned*)(S.L + L_EP + (S.lane >> 3) * 16) + TAG_STEP;
          __builtin_amdgcn_raw_buffer_store_b128(tag_bytes(S.tag) | (S.tp0 << 8), S.rws, S.lane == ((S.tpo >> 4) & 63) ? WS_TP + S.tpo : OOB, 0, AUX_SC1);
        }
      }
      if constexpr (N == KN_NP + KN_DR) { if (S.w == 0) S.tp = __builtin_amdgcn_raw_buffer_load_b128(S.rws, WS_TP + (S.tpo & ~1023) + S.lane * 16, 0, AUX_SC1); }
      if constexpr (N == NC - 1) {
        if (S.w == 0) {
          const bool ok = has_tag(S.tp, S.tag);
          const float dsum = wave_sum(ok ? (float)((int)(S.tp.x - (S.tp0 << 8)) >> 8) : 0.f), cnt = (float)__builtin_popcountll(__ballot(ok));
          const unsigned org = (S.tp0 + (int)(dsum * __builtin_amdgcn_rcpf(fmaxf(cnt, 1.f))) - sched(KN_NP)) | 1u;
          if (S.lane == 0) __hip_atomic_store(fl + 4, org, __ATOMIC_RELAXED, __HIP_MEMORY_SCOPE_WORKGROUP);
        }
      }
    }
    if constexpr (N == KN_N0) {
      STAMP(2);
      lds_wait_ge(fl + 1, 1u);
      S.q = *(LAS v4u*)(S.L + L_Q + (S.lane & 15) * 16);
      STAMP(3);
    }
    eat_ks<k_done(N - 1), k_done(N)>(S);
    if constexpr (N == soft_step()) {
      constexpr float C2 = 0.08838834764831845f * 1.4426950408889634f;
      const float s0 = S.s[0] * C2, s1 = S.s[1] * C2;
      S.m = wave_max(fmaxf(s0, s1));
      const float p0 = __builtin_amdgcn_exp2f(s0 - S.m), p1 = __builtin_amdgcn_exp2f(s1 - S.m);
      S.pp[0] = pk_bf16(p0, p0); S.pp[1] = pk_bf16(p1, p1);
      const float l = wave_sum(bf_lo(S.pp[0]) + bf_lo(S.pp[1]));
      if (S.lane == 0) *(LAS v2f*)(S.L + L_ST + S.w * 8) = v2f{S.m, l};
      lds_signal(fl + 3, S.lane);
      STAMP(4);
    }
    if constexpr (N == KN_NF) {
      lds_wait_ge(fl + 3, 8u);
      lds_wait_ge(fl + 2, 1u);
      const v2f st = *(LAS v2f*)(S.L + L_ST + (S.lane & 7) * 8);
      float M = fmaxf(st.x, dppf<XOR7>(st.x)); M = fmaxf(M, dppf<XOR2>(M)); M = fmaxf(M, dppf<XOR1>(M));
      float Ls = __builtin_amdgcn_exp2f(st.x - M) * st.y; Ls += dppf<XOR7>(Ls); Ls += dppf<XOR2>(Ls); Ls += dppf<XOR1>(Ls);
      S.fac = __builtin_amdgcn_exp2f(S.m - M) * *(LAS float*)(S.L + L_G) / Ls;
      asm volatile("" : "+v"(S.fac));
    }
    eat_vs<v_done(N - 1), v_done(N)>(S);
    SB();
    stream_step<N + 1>(S);
  }
}

__device__ __forceinline__ void to_frags(LAS unsigned char* tr, v4u* fr) {
#pragma unroll
  for (int t = 0; t < 4; ++t) {
    const v2u lo = __builtin_bit_cast(v2u, __builtin_amdgcn_ds_read_tr16_b64_v4i16((LAS v4s*)(tr + t * 32)));
    const v2u hi = __builtin_bit_cast(v2u, __builtin_amdgcn_ds_read_tr16_b64_v4i16((LAS v4s*)(tr + t * 32 + 512)));
    fr[t] = v4u{lo.x, lo.y, hi.x, hi.y};
  }
  asm volatile("" : "+v"(fr[0]), "+v"(fr[1]), "+v"(fr[2]), "+v"(fr[3])::"memory");
}
template <int I, int AUX> __device__ __forceinline__ void dma(int m0v, int voff, v4u desc, int soff) {
#define DMA(POL) asm volatile("s_mov_b32 m0, %0\n\ts_nop 0\n\tbuffer_load_dwordx4 %1, %2, %3 offen offset:%4 " POL " lds" ::"s"(m0v), "v"(voff), "s"(desc), "s"(soff - I * 1024), "i"(I * 1024) : "memory")
  if constexpr (AUX == AUX_NT) DMA("nt"); else if constexpr (AUX == AUX_SC1) DMA("sc1"); else DMA("");
#undef DMA
}
template <int N, class FQ, class FH> __device__ __forceinline__ void weight_step(int stg, LAS unsigned char* tr, int voff, v4u d2, v4u dO, v4u* fr, LAS unsigned* late, FQ& gather_q, FH& gather_h) {
  if constexpr (N < 22) {
    if constexpr (KN_PG && N == 13) PAGE();
    if constexpr (N == KN_NQ) gather_q();
    if constexpr (N == KN_NH) gather_h();
    if constexpr (N % 4 == 0) {
      wait_vm<0>();
      if constexpr (N > 0 && N <= 16) to_frags(tr + ((N / 4) & 1) * 4096, fr + N - 4);
    } else if constexpr (N % 4 == 2) {
      if (!KN_WFAST || N < 8 || lds_peek(late) == 0u) wait_vm<0>();
    }
    dma<N % 4, 0>(stg + ((N / 4 + 1) & 1) * 4096, voff, N < 16 ? dO : d2, (N < 16 ? N : N - 16) * 8192);
    SB();
    weight_step<N + 1>(stg, tr, voff, d2, dO, fr, late, gather_q, gather_h);
  }
}

__global__ void __launch_bounds__(1024, 4) __attribute__((aligned(4096)))
fused_kernel(const void* x_, const void* kc_, const void* vc_, const void* wq_, const void* w1_, const void* w3_, void* ws_, const void* wo_,
             const void* w2_, const void* gw_, void* out_) {
  __shared__ __attribute__((aligned(16))) unsigned char smem[L_BYTES];
  LAS unsigned char* L = (LAS unsigned char*)smem;
  v8ul ka;
  v2ul kb;
  unsigned long long outp;
  asm volatile("s_load_dwordx16 %0, %3, 0x0\n\ts_load_dwordx4 %1, %3, 0x40\n\ts_load_dwordx2 %2, %3, 0x50" : "=&s"(ka), "=&s"(kb), "=&s"(outp) : "s"(__builtin_amdgcn_kernarg_segment_ptr()));
  SB();
  const int tid = threadIdx.x, lane = tid & 63, w16 = __builtin_amdgcn_readfirstlane(tid >> 6), w = w16 & 7;
  const int bid = blockIdx.x, xcd = bid & 7, j = bid >> 3, h = 4 * xcd + (j & 3), b = j >> 2;
  const int r = lane >> 4, c = lane & 15;
  const bool streamer = w16 >= 8;
  const bool isq = j < 16, valid = j < 27;
  const int qm = (j - 16) >> 31;
  const int un = (11 + (qm & 5)) * xcd + (j & 15), par = un & 1, mat = (un >> 1) & 1, sl = un >> (2 + qm);
  const int ustr = 2 * FF + (qm & (8192 - 2 * FF));
  const int col0 = 64 * sl - (~qm & 32 * par);
  const int c0 = h * 43 / 8, nch = (h + 1) * 43 / 8 - c0;
  const size_t kvbase = ((size_t)(b * NH + h) * SK + (SK - WIN)) * 256;
  const int cb = col0 * 2 + (lane & 7) * 16;
  int vu = (2 * (32 * w + (lane >> 3)) + par) * ustr + cb;
  asm volatile("" : "+v"(vu));
  vu = (unsigned)cb < (unsigned)ustr ? vu : OOB;
  int vo0 = lane * 16 + w * 1024;
  asm volatile("" : "+v"(vu), "+v"(vo0));
  vo0 = streamer ? vo0 : vu;
  const int nb0 = streamer ? WIN * 256 : (valid ? DM * ustr : 0), st0 = streamer ? 8192 : 16 * ustr;
  const int m00 = streamer ? L_K0 + w * R0 * 1024 : L_STG + w * 8192;
  asm volatile("s_waitcnt lgkmcnt(0)" : "+s"(ka), "+s"(kb), "+s"(outp), "+v"(vo0) : "s"(nb0), "s"(st0), "s"(m00), "s"(kvbase));
  {
    unsigned long long p0 = mat ? ka[5] : ka[4], pk = ka[1] + kvbase;
    asm volatile("" : "+s"(p0), "+s"(pk));
    p0 = isq ? ka[3] : p0;
    PIN_S(p0);
    const v4u d0 = mk_desc(streamer ? pk : p0, nb0);
    dma<0, AUX_NT>(m00, vo0, d0, 0); dma<1, AUX_NT>(m00, vo0, d0, st0);
    SB();
    if (!streamer) {
      dma<2, AUX_NT>(m00, vo0, d0, 2 * st0); dma<3, AUX_NT>(m00, vo0, d0, 3 * st0);
      dma<0, 0>(L_X + w * 1024, (lane >> 3) * (DM * 2) + (lane & 7) * 16, mk_desc(ka[0], 8 * DM * 2), w * 128);
      unsigned long long tp = ka[6], t1 = ka[2] + kvbase, t2 = ka[7] + h * 131072, t3 = kb[0] + c0 * 8192, t5 = outp + b * DM * 2;
      int tn = w == 7 ? 1024 : 0;
      asm volatile("" : "+s"(t1), "+s"(t2), "+s"(t3), "+s"(t5), "+s"(tn));
      tp = w == 1 ? t1 : tp; PIN_S(tp);
      tp = w == 2 ? t2 : tp; PIN_S(tp);
      tp = w == 3 ? t3 : tp; PIN_S(tp);
      tp = w == 4 ? kb[1] : tp; PIN_S(tp);
      tp = w == 5 ? t5 : tp;
      tn = (unsigned)(w - 1) < 5u ? 16 : tn;
      dma<0, AUX_SC1>(w == 7 ? L_EP : L_RED + w * 2048, lane < 8 ? lane * 128 : OOB, mk_desc(tp, tn), 0);
    }
  }
  STAMP(8);
  if (tid < 8) ((LAS unsigned*)(L + L_FL))[tid] = 0u;
  barrier();
  STAMP(9);
  int role = w16 >> 3;
  PIN_S(role);
  if (role == 0) {
    LAS unsigned* fl = (LAS unsigned*)(L + L_FL);
    const rsrc_t rws = mk_rsrc((void*)ka[6], WS_BYTES);
    LAS unsigned char* tr = L + L_STG + w * 8192 + (8 * r + (c >> 2)) * 128 + (c & 3) * 8;
    {
      v4u uf[4];
      wait_vm<0>();
      to_frags(tr, uf);
      STAMP(1);
      const unsigned ps = par ? 0x07060302u : 0x05040100u;
      const v4u x0 = *(LAS v4u*)(L + L_X + w * 1024 + (c & 7) * 128 + r * 32), x1 = *(LAS v4u*)(L + L_X + w * 1024 + (c & 7) * 128 + r * 32 + 16);
      const v4u xa = {__builtin_amdgcn_perm(x0.y, x0.x, ps), __builtin_amdgcn_perm(x0.w, x0.z, ps), __builtin_amdgcn_perm(x1.y, x1.x, ps), __builtin_amdgcn_perm(x1.w, x1.z, ps)};
#pragma unroll
      for (int tt = 0; tt < 4; ++tt) {
        const v4f ua = mfma(xa, uf[tt], v4f{0.f, 0.f, 0.f, 0.f});
        if (lane < 32) *(LAS v4f*)(L + L_RED + w * 2048 + tt * 512 + lane * 16) = ua;
      }
      lds_signal(fl + 0, lane);
      STAMP(12);
      lds_wait_ge(fl + 0, 8u);
      const int ro = L_RED + r * 512 + ((w >> 2) * 16 + c) * 16 + (w & 3) * 4;
      float sum = 0.f;
#pragma unroll
      for (int ww = 0; ww < 8; ++ww) sum += *(LAS float*)(L + ro + ww * 2048);
      const int col = col0 + lane;
      const bool on = valid && !(lane & 3) && (isq || (unsigned)col < (unsigned)FF);
      __builtin_amdgcn_raw_buffer_store_b128(tag4(sum, tag_bytes(*(LAS unsigned*)(L + L_EP + w * 16) + TAG_STEP)), rws,
                                             on ? WS_PX + par * PX_HALF + (w * NCOL + (isq ? 0 : 4096 + mat * FF) + col) * 4 : OOB, 0, AUX_SC1);
      STAMP(2);
    }
    const unsigned tag = *(LAS unsigned*)(L + L_EP + b * 16) + TAG_STEP;
    auto gather_q = [&]() {
      if (w == 0) {
        v4u g;
        int po = WS_PX + (lane >> 5) * PX_HALF + (b * NCOL + h * 128 + 4 * (lane & 31)) * 4;
#pragma unroll 1
        for (unsigned spins = 0; spins < SPIN_CAP; ++spins) {
          asm volatile("" : "+v"(po)::"memory");
          g = __builtin_amdgcn_raw_buffer_load_b128(rws, po, 0, AUX_SC1);
          if (__all(has_tag(g, tag))) break;
        }
        const v4f v = untag(g);
        const v2u qq = {pk_bf16(swap32_sum(v.x), swap32_sum(v.y)), pk_bf16(swap32_sum(v.z), swap32_sum(v.w))};
        if (lane < 32) *(LAS v2u*)(L + L_Q + lane * 8) = qq;
        lds_signal(fl + 1, lane);
        STAMP(3);
      }
    };
    auto gather_h = [&]() {
      if (w == 1) {
        const v4u gx = __builtin_amdgcn_raw_buffer_load_b128(mk_rsrc((void*)ka[0], 8 * DM * 2), b * DM * 2 + lane * 16, 0, 0);
        const v4u gg = __builtin_amdgcn_raw_buffer_load_b128(mk_rsrc((void*)kb[1], DM * 2), lane * 16, 0, 0);
        const bool on = c < 2 * nch;
        int po = on ? WS_PX + (r >> 1) * PX_HALF + (b * NCOL + 4096 + (r & 1) * FF + 8 * c0 + 4 * c) * 4 : OOB;
        v4u gu;
#pragma unroll 1
        for (unsigned spins = 0; spins < SPIN_CAP; ++spins) {
          asm volatile("" : "+v"(po)::"memory");
          gu = __builtin_amdgcn_raw_buffer_load_b128(rws, po, 0, AUX_SC1);
          if (__all(!on || has_tag(gu, tag))) break;
        }
        float s = 0.f;
#pragma unroll
        for (int d = 0; d < 4; ++d) s += rbf(bf_lo(gx[d]) * bf_lo(gg[d])) + rbf(bf_hi(gx[d]) * bf_hi(gg[d]));
        s = rbf(wave_sum(s));
        const float g = rbf(1.0f / (1.0f + __expf(-s)));
        if (lane == 0) *(LAS v2f*)(L + L_G) = v2f{g, rbf(1.0f - g)};
        const v4f pv = untag(gu);
        float hv[4];
#pragma unroll
        for (int e = 0; e < 4; ++e) {
          const float t = rbf(swap32_sum(pv[e]));
          auto sw = __builtin_amdgcn_permlane16_swap(__float_as_uint(t), __float_as_uint(t), false, false);
          const float gv = __uint_as_float(sw[0]), uv = __uint_as_float(sw[1]);
          hv[e] = rbf(gv / (1.0f + __expf(-gv))) * uv;
        }
        if (lane < 16) *(LAS v2u*)(L + L_H + lane * 8) = v2u{pk_bf16(hv[0], hv[1]), pk_bf16(hv[2], hv[3])};
        lds_signal(fl + 2, lane);
        lds_wait_ge(fl + 1, 1u);
        __builtin_amdgcn_raw_buffer_store_b128(tag_bytes(tag), rws, lane == 0 ? WS_AK + (b * 32 + h) * 16 : OOB, 0, AUX_SC1);
        STAMP(13);
      }
    };
    v4u wf[16];
    weight_step<0>(L_STG + w * 8192, tr, (lane >> 3) * 1024 + w * 128 + (lane & 7) * 16, mk_desc(kb[0] + c0 * 8192, nch * 8192), mk_desc(ka[7] + h * 131072, 131072),
                   wf, fl + 3, gather_q, gather_h);
    wait_vm<0>();
    STAMP(11);
    v4f accb[4];
    {
      lds_wait_ge(fl + 2, 1u);
      v4u h0 = *(LAS v4u*)(L + L_H + r * 16), h1 = *(LAS v4u*)(L + L_H + 64 + r * 16);
      if (c) h0 = h1 = v4u{0u, 0u, 0u, 0u};
      const float omg = *(LAS float*)(L + L_G + 4);
      v4u f2[4];
      to_frags(tr + 4096, f2);
#pragma unroll
      for (int tt = 0; tt < 4; ++tt) accb[tt] = mfma(h0, f2[tt], v4f{0.f, 0.f, 0.f, 0.f});
      to_frags(tr, f2);
#pragma unroll
      for (int tt = 0; tt < 4; ++tt) accb[tt] = mfma(h1, f2[tt], accb[tt]) * omg;
    }
    int fo = (lane & 3) ? OOB : WS_FX + ((b * 32 + 4 * w + r) * 32 + h) * 64 + c * 4;
    int io = WS_FX + (b * 32 + h) * 2048 + lane * 16;
    int oo = lane < 4 ? (b * DM + 16 * h + 4 * lane) * 2 : OOB, eo = lane == 0 && h == 0 ? WS_EP + b * 128 : OOB;
    const rsrc_t rout = mk_rsrc((void*)outp, 8 * DM * 2);
    LAS unsigned char* ap = L + L_OP + (c & 7) * OPP + r * 32 + (c >> 3) * 16;
    int sidx = (c >> 3) ? 0xEEEE : 0x4444;
    v8u ww[8];
#pragma unroll
    for (int i = 0; i < 8; ++i) ww[i] = __builtin_shufflevector(wf[(i >> 2) * 8 + (i & 3)], wf[(i >> 2) * 8 + 4 + (i & 3)], 0, 1, 2, 3, 4, 5, 6, 7);
    asm volatile("" : "+v"(ww[0]), "+v"(ww[1]), "+v"(ww[2]), "+v"(ww[3]), "+v"(ww[4]), "+v"(ww[5]), "+v"(ww[6]), "+v"(ww[7]), "+v"(sidx));
    v4u tb = tag_bytes(tag);
    asm volatile("" : "+v"(fo), "+v"(io), "+v"(oo), "+v"(eo), "+v"(tb), "+v"(ap));
    asm volatile("" : "+v"(accb[0]), "+v"(accb[1]), "+v"(accb[2]), "+v"(accb[3]));
    if (w == 0) {
      const v4u t0 = __builtin_amdgcn_raw_buffer_load_b128(rws, io, 0, AUX_SC1), t1 = __builtin_amdgcn_raw_buffer_load_b128(rws, io + 1024, 0, AUX_SC1);
      asm volatile("" ::"v"(t0), "v"(t1));
    }
    if (w == 3 && h == 0) {
      int po = lane < 32 ? WS_AK + (b * 32 + lane) * 16 : OOB;
#pragma unroll 1
      for (unsigned spins = 0; spins < SPIN_CAP; ++spins) {
        asm volatile("" : "+v"(po)::"memory");
        const v4u g = __builtin_amdgcn_raw_buffer_load_b128(rws, po, 0, AUX_SC1);
        if (__all(lane >= 32 || has_tag(g, tag))) break;
      }
      __builtin_amdgcn_raw_buffer_store_b32(tag, rws, lane == 0 ? WS_EP + b * 128 : OOB, 0, AUX_SC1);
    }
    STAMP(4);
    barrier();
    STAMP(5);
    {
      const v4u oa0 = *(LAS v4u*)ap, oa1 = *(LAS v4u*)(ap + 128);
      SB();
#pragma unroll
      for (int tt = 0; tt < 4; ++tt) accb[tt] = smfma(oa0, ww[tt], accb[tt], sidx);
#pragma unroll
      for (int tt = 0; tt < 4; ++tt) accb[tt] = smfma(oa1, ww[4 + tt], accb[tt], sidx);
    }
    STAMP(10);
    {
      float sm[4];
#pragma unroll
      for (int tt = 0; tt < 4; ++tt) { sm[tt] = accb[tt].x + accb[tt].y; float t2 = accb[tt].z + accb[tt].w; asm volatile("" : "+v"(sm[tt]), "+v"(t2)); sm[tt] += t2; }
      auto s02 = __builtin_amdgcn_permlane32_swap(__float_as_uint(sm[0]), __float_as_uint(sm[2]), false, false);
      auto s13 = __builtin_amdgcn_permlane32_swap(__float_as_uint(sm[1]), __float_as_uint(sm[3]), false, false);
      auto sw = __builtin_amdgcn_permlane16_swap(__float_as_uint(__uint_as_float(s02[0]) + __uint_as_float(s02[1])), __float_as_uint(__uint_as_float(s13[0]) + __uint_as_float(s13[1])), false, false);
      __builtin_amdgcn_raw_buffer_store_b128(tag4(__uint_as_float(sw[0]) + __uint_as_float(sw[1]), tb), rws, fo, 0, AUX_SC1);
    }
    STAMP(6);
    if (w == 0) {
      v4f y4 = {0.f, 0.f, 0.f, 0.f};
      int o0 = io, o1 = io;
      __builtin_amdgcn_s_sleep(KN_PD);
#pragma unroll 1
      for (unsigned spins = 0; spins < SPIN_CAP; ++spins) {
        asm volatile("" : "+v"(o0), "+v"(o1)::"memory");
        const v4u g0 = __builtin_amdgcn_raw_buffer_load_b128(rws, o0, 0, AUX_SC1), g1 = __builtin_amdgcn_raw_buffer_load_b128(rws, o1 + 1024, 0, AUX_SC1);
        const bool got0 = o0 < OOB && has_tag(g0, tag), got1 = o1 < OOB && has_tag(g1, tag);
        y4 += got0 ? untag(g0) : v4f{0.f, 0.f, 0.f, 0.f};
        y4 += got1 ? untag(g1) : v4f{0.f, 0.f, 0.f, 0.f};
        o0 = got0 ? OOB : o0; o1 = got1 ? OOB : o1;
        if (__all(o0 >= OOB && o1 >= OOB)) break;
      }
#pragma unroll
      for (int e = 0; e < 4; ++e) { float t = y4[e]; t += dppf<XOR8>(t); t += dppf<ROR4>(t); y4[e] = rows_sum(t); }
      __builtin_amdgcn_raw_buffer_store_b64(v2u{pk_bf16(y4.x, y4.y), pk_bf16(y4.z, y4.w)}, rout, oo, 0, 0);
      STAMP(7);
    }
    STAMP_OUT();
    __builtin_amdgcn_endpgm();
  } else {
    Stream S;
    S.rk = mk_rsrc((void*)(ka[1] + kvbase), WIN * 256); S.rv = mk_rsrc((void*)(ka[2] + kvbase), WIN * 256); S.rws = mk_rsrc((void*)ka[6], WS_BYTES);
    S.voff = lane * 16 + w * 1024; S.lane = lane; S.L = L; S.w = w; S.tpo = ((j & 3) * 64 + b * 8 + xcd) * 16;
    S.acc = v4f{0.f, 0.f, 0.f, 0.f};
#pragma unroll
    for (int d = 0; d < 4; ++d) S.msk[d] = (c >> 1) == d ? ((c & 1) ? 0xffff0000u : 0x0000ffffu) : 0u;
    stream_step<R0>(S);
    LAS unsigned* op = (LAS unsigned*)(L + L_OP + w * OPP) + 8 * (c >> 1) + ((2 * c + r) & 3);
    asm volatile("" : "+v"(op));
    eat_vs<v_done(63), 32>(S);
    STAMP(5);
    if (lane < 32) {
      op[0] = pk_bf16(S.acc.x * S.fac, S.acc.y * S.fac);
      op[4] = pk_bf16(S.acc.z * S.fac, S.acc.w * S.fac);
    }
    barrier();
    STAMP_OUT();
    if (KN_PG) asm volatile("s_endpgm\n\t.p2align 12");
    else asm volatile("s_endpgm");
    __builtin_unreachable();
  }
}

}

extern "C" void kernel_launch(void* const* d_in, const int* in_sizes, int n_in, void* d_out, int out_size, void* d_ws, size_t ws_size,
                              hipStream_t stream) {
  fused_kernel<<<256, 1024, 0, stream>>>(d_in[0], d_in[1], d_in[2], d_in[3], d_in[6], d_in[7], d_ws, d_in[4], d_in[8], d_in[9], d_out);
}
```

```cpp
#include <hip/hip_runtime.h>
#include <hip/hip_bf16.h>

namespace {

typedef unsigned v4u __attribute__((ext_vector_type(4)));
typedef unsigned v2u __attribute__((ext_vector_type(2)));
typedef unsigned v8u __attribute__((ext_vector_type(8)));
typedef __bf16 v16bf __attribute__((ext_vector_type(16)));
typedef float v4f __attribute__((ext_vector_type(4)));
typedef float v2f __attribute__((ext_vector_type(2)));
typedef short v4s __attribute__((ext_vector_type(4)));
typedef __bf16 v8bf __attribute__((ext_vector_type(8)));
typedef __bf16 v2bf __attribute__((ext_vector_type(2)));
typedef unsigned long long v8ul __attribute__((ext_vector_type(8)));
typedef unsigned long long v2ul __attribute__((ext_vector_type(2)));
#define LAS __attribute__((address_space(3)))
typedef __amdgpu_buffer_rsrc_t rsrc_t;

constexpr int DM = 512, NH = 32, SK = 4096, WIN = 1024, FF = 1376;
constexpr int NCOL = 4096 + 2 * FF;
constexpr int WS_EP = 0, WS_PX = 4096, PX_HALF = 8 * NCOL * 4, WS_TP = 480 * 1024, WS_AK = 488 * 1024, WS_FX = 512 * 1024, WS_BYTES = WS_FX + 8 * 32 * 32 * 16 * 4;
constexpr int AUX_NT = 2, AUX_SC1 = 16, OOB = 0x40000000;
constexpr unsigned TAG_STEP = 0x01010101u;
constexpr unsigned SPIN_CAP = 1u << 16;
constexpr int R0 = 2;
constexpr int R = 3;
constexpr int RHI = 5, THI = 25;
constexpr int RLO = 2, TLO = 30;
constexpr int NP = 20, DR = 7;
constexpr int TA = 20, DT = 498, DT2 = 474, NB = 44;
constexpr int N0 = 20;
constexpr int NF = 52;
constexpr int NQ = 2, NG = 8;
constexpr int PD = 16;
constexpr int L_STG = 0;
constexpr int L_K0 = 65536;
constexpr int L_RED = L_K0 + 8 * R0 * 1024;
constexpr int L_X = L_RED + 16384;
constexpr int OPP = 320;
constexpr int L_OP = L_X + 8192;
constexpr int L_EP = L_OP + 8 * OPP;
constexpr int L_Q = L_EP + 1024;
constexpr int L_H = L_Q + 256;
constexpr int L_ST = L_H + 128;
constexpr int L_G = L_ST + 64;
constexpr int L_FL = L_G + 16;
constexpr int L_BYTES = L_FL + 32;

__device__ __forceinline__ rsrc_t mk_rsrc(const void* p, int bytes) { return __builtin_amdgcn_make_buffer_rsrc((void*)p, 0, bytes, 0x00020000); }
__device__ __forceinline__ v4u mk_desc(unsigned long long a, int bytes) { return v4u{(unsigned)a, (unsigned)(a >> 32) & 0xffffu, (unsigned)bytes, 0x00020000u}; }
__device__ __forceinline__ unsigned pk_bf16(float lo, float hi) { v2bf r = {(__bf16)lo, (__bf16)hi}; return __builtin_bit_cast(unsigned, r); }
__device__ __forceinline__ float rbf(float x) { return (float)(__bf16)x; }
__device__ __forceinline__ float bf_lo(unsigned u) { return __builtin_bit_cast(float, u << 16); }
__device__ __forceinline__ float bf_hi(unsigned u) { return __builtin_bit_cast(float, u & 0xffff0000u); }
__device__ __forceinline__ float dot2(unsigned a, unsigned b, float c) { return __builtin_amdgcn_fdot2_f32_bf16(__builtin_bit_cast(v2bf, a), __builtin_bit_cast(v2bf, b), c, false); }
__device__ __forceinline__ v4f mfma(v4u a, v4u b, v4f c) { return __builtin_amdgcn_mfma_f32_16x16x32_bf16(__builtin_bit_cast(v8bf, a), __builtin_bit_cast(v8bf, b), c, 0, 0, 0); }
__device__ __forceinline__ v4f smfma(v4u a, v8u b, v4f c, int idx) { return __builtin_amdgcn_smfmac_f32_16x16x64_bf16(__builtin_bit_cast(v8bf, a), __builtin_bit_cast(v16bf, b), c, idx, 0, 0); }
template <int CTRL> __device__ __forceinline__ unsigned dppu(unsigned x) { return (unsigned)__builtin_amdgcn_mov_dpp((int)x, CTRL, 0xf, 0xf, true); }
template <int CTRL> __device__ __forceinline__ float dppf(float x) { return __builtin_bit_cast(float, dppu<CTRL>(__builtin_bit_cast(unsigned, x))); }
constexpr int XOR1 = 0xB1, XOR2 = 0x4E, XOR7 = 0x141, XOR8 = 0x128, ROR4 = 0x124, BCAST = 0x150;
__device__ __forceinline__ float swap32_sum(float x) {
  auto t = __builtin_amdgcn_permlane32_swap(__float_as_uint(x), __float_as_uint(x), false, false);
  return __uint_as_float(t[0]) + __uint_as_float(t[1]);
}
__device__ __forceinline__ float rows_sum(float x) {
  auto s = __builtin_amdgcn_permlane16_swap(__float_as_uint(x), __float_as_uint(x), false, false);
  return swap32_sum(__uint_as_float(s[0]) + __uint_as_float(s[1]));
}
__device__ __forceinline__ float rows_max(float x) {
  auto s = __builtin_amdgcn_permlane16_swap(__float_as_uint(x), __float_as_uint(x), false, false);
  x = fmaxf(__uint_as_float(s[0]), __uint_as_float(s[1]));
  auto t = __builtin_amdgcn_permlane32_swap(__float_as_uint(x), __float_as_uint(x), false, false);
  return fmaxf(__uint_as_float(t[0]), __uint_as_float(t[1]));
}
__device__ __forceinline__ float wave_sum(float x) { x += dppf<XOR8>(x); x += dppf<XOR7>(x); x += dppf<XOR2>(x); x += dppf<XOR1>(x); return rows_sum(x); }
__device__ __forceinline__ float wave_max(float x) { x = fmaxf(x, dppf<XOR8>(x)); x = fmaxf(x, dppf<XOR7>(x)); x = fmaxf(x, dppf<XOR2>(x)); x = fmaxf(x, dppf<XOR1>(x)); return rows_max(x); }
template <int N> __device__ __forceinline__ void wait_vm() { asm volatile("s_waitcnt vmcnt(%0)" ::"i"(N) : "memory"); }
__device__ __forceinline__ void barrier() { asm volatile("s_waitcnt lgkmcnt(0)\n\ts_barrier" ::: "memory"); }
__device__ __forceinline__ unsigned lds_peek(LAS unsigned* p) { return __builtin_amdgcn_readfirstlane(__hip_atomic_load(p, __ATOMIC_RELAXED, __HIP_MEMORY_SCOPE_WORKGROUP)); }
__device__ __forceinline__ void lds_wait_ge(LAS unsigned* p, unsigned want) {
  while (lds_peek(p) < want) __builtin_amdgcn_s_sleep(2);
  asm volatile("" ::: "memory");
}
__device__ __forceinline__ void lds_signal(LAS unsigned* p, int lane) {
  asm volatile("s_waitcnt lgkmcnt(0)" ::: "memory");
  if (lane == 0) __hip_atomic_fetch_add(p, 1u, __ATOMIC_RELAXED, __HIP_MEMORY_SCOPE_WORKGROUP);
  asm volatile("" ::: "memory");
}
__device__ __forceinline__ v4u tag_bytes(unsigned tag) { return v4u{tag & 0xffu, (tag >> 8) & 0xffu, (tag >> 16) & 0xffu, tag >> 24}; }
__device__ __forceinline__ v4u tag4(float y, v4u tb) {
  const unsigned u = (__float_as_uint(y) + 0x80u) & 0xffffff00u;
  return v4u{u | tb.x, dppu<0x55>(u) | tb.y, dppu<0xAA>(u) | tb.z, dppu<0xFF>(u) | tb.w};
}
__device__ __forceinline__ bool has_tag(v4u g, unsigned tag) {
  return __builtin_amdgcn_perm(__builtin_amdgcn_perm(g.w, g.z, 0x0c0c0400u), __builtin_amdgcn_perm(g.y, g.x, 0x0c0c0400u), 0x05040100u) == tag;
}
__device__ __forceinline__ v4f untag(v4u g) { return __builtin_bit_cast(v4f, g & 0xffffff00u); }
#define PAGE(n) asm volatile("s_branch .Lgm_after" #n "\n\t.p2align 12\n.Lgm_page" #n ":\n\ts_setpc_b64 s[30:31]\n.Lgm_after" #n ":" ::: "memory")
#define VISIT(n) asm volatile("s_getpc_b64 s[28:29]\n.Lgm_from" #n ":\n\ts_add_u32 s28, s28, (.Lgm_page" #n "-.Lgm_from" #n ")&0xffffffff\n\t" \
                              "s_addc_u32 s29, s29, (.Lgm_page" #n "-.Lgm_from" #n ")>>32\n\ts_swappc_b64 s[30:31], s[28:29]" ::: "s28", "s29", "s30", "s31", "scc", "memory")
#define SB() __builtin_amdgcn_sched_barrier(0)
#define PIN_S(x) asm volatile("" : "+s"(x))
constexpr int rev4(int i) { return ((i & 1) << 3) | ((i & 2) << 1) | ((i & 4) >> 1) | ((i & 8) >> 3); }

struct Stream {
  rsrc_t rk, rv, rws;
  int voff, so, lane, w, tpo, lag;
  LAS unsigned char* L;
  v4u a[64], q, tp;
  float lv[4], s[2], m, fac;
  unsigned pp[2], msk[4], tag, tp0;
  v4f acc;
};
constexpr int k_done(int n) { int d = n < N0 ? 0 : 2 * (n - N0 + 1); int lim = n - RHI + 1; d = d < lim ? d : lim; return d < 0 ? 0 : (d > 32 ? 32 : d); }
constexpr int soft_step() { int n = 0; while (k_done(n) < 32) ++n; return n + 1; }
constexpr int v_done(int n) { int d = n <= soft_step() ? 0 : 2 * (n - soft_step()); int lim = n - RHI + 1 - 32; d = d < lim ? d : lim; return d < 0 ? 0 : (d > 32 ? 32 : d); }
constexpr int sched(int n) { return ((n < NB ? n : NB) * DT + (n < NB ? 0 : n - NB) * DT2) / 16; }
constexpr int NC = NP + DR + RHI + 1;

template <int I> __device__ __forceinline__ void eat_k(Stream& S) {
  if constexpr (I < R0) S.a[I] = *(LAS v4u*)(S.L + L_K0 + (S.w * R0 + I) * 1024 + S.lane * 16);
  float d = dot2(S.a[I].x, S.q.x, 0.f);
  d = dot2(S.a[I].y, S.q.y, d); d = dot2(S.a[I].z, S.q.z, d); d = dot2(S.a[I].w, S.q.w, d);
  const int c = S.lane;
#define TR_LEVEL(LV, CTRL, BIT) \
  if constexpr ((I & (1 << LV)) == 0) { S.lv[LV] = d; return; } \
  { const float t1 = S.lv[LV] + dppf<CTRL>(S.lv[LV]), t2 = d + dppf<CTRL>(d); d = (c & BIT) ? t2 : t1; }
  TR_LEVEL(0, XOR8, 8) TR_LEVEL(1, XOR7, 4) TR_LEVEL(2, XOR2, 2) TR_LEVEL(3, XOR1, 1)
#undef TR_LEVEL
  S.s[I >> 4] = d;
}
template <int I> __device__ __forceinline__ void eat_v(Stream& S) {
  const unsigned t = dppu<BCAST + rev4(I & 15)>(S.pp[I >> 4]);
  const v4u cf = {t & S.msk[0], t & S.msk[1], t & S.msk[2], t & S.msk[3]};
  S.acc = mfma(cf, S.a[32 + I], S.acc);
}
template <int LO, int HI> __device__ __forceinline__ void eat_ks(Stream& S) { if constexpr (LO < HI) { eat_k<LO>(S); eat_ks<LO + 1, HI>(S); } }
template <int LO, int HI> __device__ __forceinline__ void eat_vs(Stream& S) { if constexpr (LO < HI) { eat_v<LO>(S); SB(); eat_vs<LO + 1, HI>(S); } }

template <int N> __device__ __forceinline__ void stream_step(Stream& S) {
  if constexpr (N < 64) {
    LAS unsigned* fl = (LAS unsigned*)(S.L + L_FL);
    if constexpr (N == 37) PAGE(1);
    if constexpr (N >= NC) {
      if constexpr ((N - NC) % 3 == 0) {
        const unsigned org = lds_peek(fl + 4);
        const int lag = (int)((unsigned)__builtin_amdgcn_s_memrealtime() - org) - (TA + sched(N));
        S.lag = org ? lag : -1;
      }
      asm volatile("s_cmpk_gt_i32 %0, %1\n\ts_cbranch_scc1 1f\n\ts_cmpk_gt_i32 %0, %2\n\ts_cbranch_scc1 2f\n\t"
                   "s_waitcnt vmcnt(%3)\n2:\n\ts_waitcnt vmcnt(%4)\n1:\n\ts_waitcnt vmcnt(%5)"
                   ::"s"(S.lag), "i"(THI), "i"(-TLO & 0xffff), "i"(RLO - 1), "i"(R - 1), "i"(RHI - 1) : "memory", "scc");
    } else if constexpr (N >= R) wait_vm<R - 1>();
    if constexpr (N == 32) S.so = 0;
    S.a[N] = __builtin_amdgcn_raw_buffer_load_b128(N < 32 ? S.rk : S.rv, S.voff, S.so, AUX_NT);
    SB();
    if constexpr (N != 31 && N != 63) asm volatile("s_addk_i32 %0, 0x2000" : "+s"(S.so)::"scc");
    {
      if constexpr (N == NP) {
        if (S.w == 0) {
          S.tp0 = (unsigned)__builtin_amdgcn_s_memrealtime();
          lds_wait_ge(fl + 0, 8u);
          S.tag = *(LAS unsigned*)(S.L + L_EP + (S.lane >> 3) * 16) + TAG_STEP;
          __builtin_amdgcn_raw_buffer_store_b128(tag_bytes(S.tag) | (S.tp0 << 8), S.rws, S.lane == ((S.tpo >> 4) & 63) ? WS_TP + S.tpo : OOB, 0, AUX_SC1);
        }
      }
      if constexpr (N == NP + DR) { if (S.w == 0) S.tp = __builtin_amdgcn_raw_buffer_load_b128(S.rws, WS_TP + (S.tpo & ~1023) + S.lane * 16, 0, AUX_SC1); }
      if constexpr (N == NC - 1) {
        if (S.w == 0) {
          const bool ok = has_tag(S.tp, S.tag);
          const float dsum = wave_sum(ok ? (float)((int)(S.tp.x - (S.tp0 << 8)) >> 8) : 0.f), cnt = (float)__builtin_popcountll(__ballot(ok));
          const unsigned org = (S.tp0 + (int)(dsum * __builtin_amdgcn_rcpf(fmaxf(cnt, 1.f))) - sched(NP)) | 1u;
          if (S.lane == 0) __hip_atomic_store(fl + 4, org, __ATOMIC_RELAXED, __HIP_MEMORY_SCOPE_WORKGROUP);
        }
      }
    }
    if constexpr (N == N0) {
      lds_wait_ge(fl + 1, 1u);
      S.q = *(LAS v4u*)(S.L + L_Q + (S.lane & 15) * 16);
    }
    eat_ks<k_done(N - 1), k_done(N)>(S);
    if constexpr (N == soft_step()) {
      constexpr float C2 = 0.08838834764831845f * 1.4426950408889634f;
      const float s0 = S.s[0] * C2, s1 = S.s[1] * C2;
      S.m = wave_max(fmaxf(s0, s1));
      const float p0 = __builtin_amdgcn_exp2f(s0 - S.m), p1 = __builtin_amdgcn_exp2f(s1 - S.m);
      S.pp[0] = pk_bf16(p0, p0); S.pp[1] = pk_bf16(p1, p1);
      const float l = wave_sum(bf_lo(S.pp[0]) + bf_lo(S.pp[1]));
      if (S.lane == 0) *(LAS v2f*)(S.L + L_ST + S.w * 8) = v2f{S.m, l};
      lds_signal(fl + 3, S.lane);
    }
    if constexpr (N == NF) {
      lds_wait_ge(fl + 3, 8u);
      lds_wait_ge(fl + 2, 1u);
      const v2f st = *(LAS v2f*)(S.L + L_ST + (S.lane & 7) * 8);
      float M = fmaxf(st.x, dppf<XOR7>(st.x)); M = fmaxf(M, dppf<XOR2>(M)); M = fmaxf(M, dppf<XOR1>(M));
      float Ls = __builtin_amdgcn_exp2f(st.x - M) * st.y; Ls += dppf<XOR7>(Ls); Ls += dppf<XOR2>(Ls); Ls += dppf<XOR1>(Ls);
      S.fac = __builtin_amdgcn_exp2f(S.m - M) * *(LAS float*)(S.L + L_G) / Ls;
      asm volatile("" : "+v"(S.fac));
    }
    eat_vs<v_done(N - 1), v_done(N)>(S);
    SB();
    stream_step<N + 1>(S);
  }
}

__device__ __forceinline__ void to_frags(LAS unsigned char* tr, v4u* fr) {
#pragma unroll
  for (int t = 0; t < 4; ++t) {
    const v2u lo = __builtin_bit_cast(v2u, __builtin_amdgcn_ds_read_tr16_b64_v4i16((LAS v4s*)(tr + t * 32)));
    const v2u hi = __builtin_bit_cast(v2u, __builtin_amdgcn_ds_read_tr16_b64_v4i16((LAS v4s*)(tr + t * 32 + 512)));
    fr[t] = v4u{lo.x, lo.y, hi.x, hi.y};
  }
  asm volatile("" : "+v"(fr[0]), "+v"(fr[1]), "+v"(fr[2]), "+v"(fr[3])::"memory");
}
template <int I, int AUX> __device__ __forceinline__ void dma(int m0v, int voff, v4u desc, int soff) {
#define DMA(POL) asm volatile("s_mov_b32 m0, %0\n\ts_nop 0\n\tbuffer_load_dwordx4 %1, %2, %3 offen offset:%4 " POL " lds" ::"s"(m0v), "v"(voff), "s"(desc), "s"(soff - I * 1024), "i"(I * 1024) : "memory")
  if constexpr (AUX == AUX_NT) DMA("nt"); else if constexpr (AUX == AUX_SC1) DMA("sc1"); else DMA("");
#undef DMA
}
template <int N, class FQ, class FH> __device__ __forceinline__ void weight_step(int stg, LAS unsigned char* tr, int voff, v4u d2, v4u dO, v4u* fr, LAS unsigned* late, FQ& gather_q, FH& gather_h) {
  if constexpr (N < 22) {
    if constexpr (N == 13) PAGE(2);
    if constexpr (N == NQ) gather_q();
    if constexpr (N == NG) gather_h();
    if constexpr (N % 4 == 0) {
      wait_vm<0>();
      if constexpr (N > 0 && N <= 16) to_frags(tr + ((N / 4) & 1) * 4096, fr + N - 4);
    } else if constexpr (N % 4 == 2) {
      if (N < 8 || lds_peek(late) == 0u) wait_vm<0>();
    }
    dma<N % 4, 0>(stg + ((N / 4 + 1) & 1) * 4096, voff, N < 16 ? dO : d2, (N < 16 ? N : N - 16) * 8192);
    SB();
    weight_step<N + 1>(stg, tr, voff, d2, dO, fr, late, gather_q, gather_h);
  }
}

__global__ void __launch_bounds__(1024, 4) __attribute__((aligned(4096)))
fused_kernel(const void* x_, const void* kc_, const void* vc_, const void* wq_, const void* w1_, const void* w3_, void* ws_, const void* wo_,
             const void* w2_, const void* gw_, void* out_) {
  __shared__ __attribute__((aligned(16))) unsigned char smem[L_BYTES];
  LAS unsigned char* L = (LAS unsigned char*)smem;
  v8ul ka;
  v2ul kb;
  unsigned long long outp;
  asm volatile("s_load_dwordx16 %0, %3, 0x0\n\ts_load_dwordx4 %1, %3, 0x40\n\ts_load_dwordx2 %2, %3, 0x50" : "=&s"(ka), "=&s"(kb), "=&s"(outp) : "s"(__builtin_amdgcn_kernarg_segment_ptr()));
  SB();
  const int tid = threadIdx.x, lane = tid & 63, w16 = __builtin_amdgcn_readfirstlane(tid >> 6), w = w16 & 7;
  const int bid = blockIdx.x, xcd = bid & 7, j = bid >> 3, h = 4 * xcd + (j & 3), b = j >> 2;
  const int r = lane >> 4, c = lane & 15;
  const bool streamer = w16 >= 8;
  const bool isq = j < 16, valid = j < 27;
  const int qm = (j - 16) >> 31;
  const int un = (11 + (qm & 5)) * xcd + (j & 15), par = un & 1, mat = (un >> 1) & 1, sl = un >> (2 + qm);
  const int ustr = 2 * FF + (qm & (8192 - 2 * FF));
  const int col0 = 64 * sl - (~qm & 32 * par);
  const int c0 = h * 43 / 8, nch = (h + 1) * 43 / 8 - c0;
  const size_t kvbase = ((size_t)(b * NH + h) * SK + (SK - WIN)) * 256;
  const int cb = col0 * 2 + (lane & 7) * 16;
  int vu = (2 * (32 * w + (lane >> 3)) + par) * ustr + cb;
  asm volatile("" : "+v"(vu));
  vu = (unsigned)cb < (unsigned)ustr ? vu : OOB;
  int vo0 = lane * 16 + w * 1024;
  asm volatile("" : "+v"(vu), "+v"(vo0));
  vo0 = streamer ? vo0 : vu;
  const int nb0 = streamer ? WIN * 256 : (valid ? DM * ustr : 0), st0 = streamer ? 8192 : 16 * ustr;
  const int m00 = streamer ? L_K0 + w * R0 * 1024 : L_STG + w * 8192;
  asm volatile("s_waitcnt lgkmcnt(0)" : "+s"(ka), "+s"(kb), "+s"(outp), "+v"(vo0) : "s"(nb0), "s"(st0), "s"(m00), "s"(kvbase));
  {
    unsigned long long p0 = mat ? ka[5] : ka[4], pk = ka[1] + kvbase;
    asm volatile("" : "+s"(p0), "+s"(pk));
    p0 = isq ? ka[3] : p0;
    PIN_S(p0);
    const v4u d0 = mk_desc(streamer ? pk : p0, nb0);
    dma<0, AUX_NT>(m00, vo0, d0, 0); dma<1, AUX_NT>(m00, vo0, d0, st0);
    SB();
    if (!streamer) {
      dma<2, AUX_NT>(m00, vo0, d0, 2 * st0); dma<3, AUX_NT>(m00, vo0, d0, 3 * st0);
      dma<0, 0>(L_X + w * 1024, (lane >> 3) * (DM * 2) + (lane & 7) * 16, mk_desc(ka[0], 8 * DM * 2), w * 128);
      unsigned long long tp = ka[6], t1 = ka[2] + kvbase, t2 = ka[7] + h * 131072, t3 = kb[0] + c0 * 8192, t5 = outp + (b * DM + 16 * h) * 2;
      int tn = w == 7 ? 1024 : 0;
      asm volatile("" : "+s"(t1), "+s"(t2), "+s"(t3), "+s"(t5), "+s"(tn));
      tp = w == 1 ? t1 : tp; PIN_S(tp);
      tp = w == 2 ? t2 : tp; PIN_S(tp);
      tp = w == 3 ? t3 : tp; PIN_S(tp);
      tp = w == 4 ? kb[1] : tp; PIN_S(tp);
      tp = w == 5 ? t5 : tp;
      tn = (unsigned)(w - 1) < 5u ? 16 : tn;
      dma<0, AUX_SC1>(w == 7 ? L_EP : L_RED + w * 2048, lane < 8 ? lane * 128 : OOB, mk_desc(tp, tn), 0);
    }
  }
  if (tid < 8) ((LAS unsigned*)(L + L_FL))[tid] = 0u;
  barrier();
  int role = w16 >> 3;
  PIN_S(role);
  if (role == 0) {
    LAS unsigned* fl = (LAS unsigned*)(L + L_FL);
    const rsrc_t rws = mk_rsrc((void*)ka[6], WS_BYTES);
    LAS unsigned char* tr = L + L_STG + w * 8192 + (8 * r + (c >> 2)) * 128 + (c & 3) * 8;
    {
      v4u uf[4];
      wait_vm<0>();
      to_frags(tr, uf);
      const unsigned ps = par ? 0x07060302u : 0x05040100u;
      const v4u x0 = *(LAS v4u*)(L + L_X + w * 1024 + (c & 7) * 128 + r * 32), x1 = *(LAS v4u*)(L + L_X + w * 1024 + (c & 7) * 128 + r * 32 + 16);
      const v4u xa = {__builtin_amdgcn_perm(x0.y, x0.x, ps), __builtin_amdgcn_perm(x0.w, x0.z, ps), __builtin_amdgcn_perm(x1.y, x1.x, ps), __builtin_amdgcn_perm(x1.w, x1.z, ps)};
#pragma unroll
      for (int tt = 0; tt < 4; ++tt) {
        const v4f ua = mfma(xa, uf[tt], v4f{0.f, 0.f, 0.f, 0.f});
        if (lane < 32) *(LAS v4f*)(L + L_RED + w * 2048 + tt * 512 + lane * 16) = ua;
      }
      lds_signal(fl + 0, lane);
      lds_wait_ge(fl + 0, 8u);
      const int ro = L_RED + r * 512 + ((w >> 2) * 16 + c) * 16 + (w & 3) * 4;
      float sum = 0.f;
#pragma unroll
      for (int ww = 0; ww < 8; ++ww) sum += *(LAS float*)(L + ro + ww * 2048);
      const int col = col0 + lane;
      const bool on = valid && !(lane & 3) && (isq || (unsigned)col < (unsigned)FF);
      __builtin_amdgcn_raw_buffer_store_b128(tag4(sum, tag_bytes(*(LAS unsigned*)(L + L_EP + w * 16) + TAG_STEP)), rws,
                                             on ? WS_PX + par * PX_HALF + (w * NCOL + (isq ? 0 : 4096 + mat * FF) + col) * 4 : OOB, 0, AUX_SC1);
    }
    if (w == 4) VISIT(1);
    if (w == 5) VISIT(2);
    const unsigned tag = *(LAS unsigned*)(L + L_EP + b * 16) + TAG_STEP;
    auto gather_q = [&]() {
      if (w == 0) {
        v4u g;
        int po = WS_PX + (lane >> 5) * PX_HALF + (b * NCOL + h * 128 + 4 * (lane & 31)) * 4;
#pragma unroll 1
        for (unsigned spins = 0; spins < SPIN_CAP; ++spins) {
          asm volatile("" : "+v"(po)::"memory");
          g = __builtin_amdgcn_raw_buffer_load_b128(rws, po, 0, AUX_SC1);
          if (__all(has_tag(g, tag))) break;
        }
        const v4f v = untag(g);
        const v2u qq = {pk_bf16(swap32_sum(v.x), swap32_sum(v.y)), pk_bf16(swap32_sum(v.z), swap32_sum(v.w))};
        if (lane < 32) *(LAS v2u*)(L + L_Q + lane * 8) = qq;
        lds_signal(fl + 1, lane);
      }
    };
    auto gather_h = [&]() {
      if (w == 1) {
        const v4u gx = __builtin_amdgcn_raw_buffer_load_b128(mk_rsrc((void*)ka[0], 8 * DM * 2), b * DM * 2 + lane * 16, 0, 0);
        const v4u gg = __builtin_amdgcn_raw_buffer_load_b128(mk_rsrc((void*)kb[1], DM * 2), lane * 16, 0, 0);
        const bool on = c < 2 * nch;
        int po = on ? WS_PX + (r >> 1) * PX_HALF + (b * NCOL + 4096 + (r & 1) * FF + 8 * c0 + 4 * c) * 4 : OOB;
        v4u gu;
#pragma unroll 1
        for (unsigned spins = 0; spins < SPIN_CAP; ++spins) {
          asm volatile("" : "+v"(po)::"memory");
          gu = __builtin_amdgcn_raw_buffer_load_b128(rws, po, 0, AUX_SC1);
          if (__all(!on || has_tag(gu, tag))) break;
        }
        float s = 0.f;
#pragma unroll
        for (int d = 0; d < 4; ++d) s += rbf(bf_lo(gx[d]) * bf_lo(gg[d])) + rbf(bf_hi(gx[d]) * bf_hi(gg[d]));
        s = rbf(wave_sum(s));
        const float g = rbf(1.0f / (1.0f + __expf(-s)));
        if (lane == 0) *(LAS v2f*)(L + L_G) = v2f{g, rbf(1.0f - g)};
        const v4f pv = untag(gu);
        float hv[4];
#pragma unroll
        for (int e = 0; e < 4; ++e) {
          const float t = rbf(swap32_sum(pv[e]));
          auto sw = __builtin_amdgcn_permlane16_swap(__float_as_uint(t), __float_as_uint(t), false, false);
          const float gv = __uint_as_float(sw[0]), uv = __uint_as_float(sw[1]);
          hv[e] = rbf(gv / (1.0f + __expf(-gv))) * uv;
        }
        if (lane < 16) *(LAS v2u*)(L + L_H + lane * 8) = v2u{pk_bf16(hv[0], hv[1]), pk_bf16(hv[2], hv[3])};
        lds_signal(fl + 2, lane);
        lds_wait_ge(fl + 1, 1u);
        __builtin_amdgcn_raw_buffer_store_b128(tag_bytes(tag), rws, lane == 0 ? WS_AK + (b * 32 + h) * 16 : OOB, 0, AUX_SC1);
      }
    };
    v4u wf[16];
    weight_step<0>(L_STG + w * 8192, tr, (lane >> 3) * 1024 + w * 128 + (lane & 7) * 16, mk_desc(kb[0] + c0 * 8192, nch * 8192), mk_desc(ka[7] + h * 131072, 131072),
                   wf, fl + 3, gather_q, gather_h);
    wait_vm<0>();
    v4f accb[4];
    {
      lds_wait_ge(fl + 2, 1u);
      v4u h0 = *(LAS v4u*)(L + L_H + r * 16), h1 = *(LAS v4u*)(L + L_H + 64 + r * 16);
      if (c) h0 = h1 = v4u{0u, 0u, 0u, 0u};
      const float omg = *(LAS float*)(L + L_G + 4);
      v4u f2[4];
      to_frags(tr + 4096, f2);
#pragma unroll
      for (int tt = 0; tt < 4; ++tt) accb[tt] = mfma(h0, f2[tt], v4f{0.f, 0.f, 0.f, 0.f});
      to_frags(tr, f2);
#pragma unroll
      for (int tt = 0; tt < 4; ++tt) accb[tt] = mfma(h1, f2[tt], accb[tt]) * omg;
    }
    int fo = (lane & 3) ? OOB : WS_FX + ((b * 32 + 4 * w + r) * 32 + h) * 64 + c * 4;
    int io = WS_FX + (b * 32 + h) * 2048 + lane * 16;
    int oo = lane < 4 ? (b * DM + 16 * h + 4 * lane) * 2 : OOB;
    const rsrc_t rout = mk_rsrc((void*)outp, 8 * DM * 2);
    LAS unsigned char* ap = L + L_OP + (c & 7) * OPP + r * 32 + (c >> 3) * 16;
    int sidx = (c >> 3) ? 0xEEEE : 0x4444;
    v8u ww[8];
#pragma unroll
    for (int i = 0; i < 8; ++i) ww[i] = __builtin_shufflevector(wf[(i >> 2) * 8 + (i & 3)], wf[(i >> 2) * 8 + 4 + (i & 3)], 0, 1, 2, 3, 4, 5, 6, 7);
    asm volatile("" : "+v"(ww[0]), "+v"(ww[1]), "+v"(ww[2]), "+v"(ww[3]), "+v"(ww[4]), "+v"(ww[5]), "+v"(ww[6]), "+v"(ww[7]), "+v"(sidx));
    v4u tb = tag_bytes(tag);
    asm volatile("" : "+v"(fo), "+v"(io), "+v"(oo), "+v"(tb), "+v"(ap));
    asm volatile("" : "+v"(accb[0]), "+v"(accb[1]), "+v"(accb[2]), "+v"(accb[3]));
    if (w == 0) {
      const v4u t0 = __builtin_amdgcn_raw_buffer_load_b128(rws, io, 0, AUX_SC1), t1 = __builtin_amdgcn_raw_buffer_load_b128(rws, io + 1024, 0, AUX_SC1);
      asm volatile("" ::"v"(t0), "v"(t1));
    }
    if (w == 3 && h == 0) {
      int po = lane < 32 ? WS_AK + (b * 32 + lane) * 16 : OOB;
#pragma unroll 1
      for (unsigned spins = 0; spins < SPIN_CAP; ++spins) {
        asm volatile("" : "+v"(po)::"memory");
        const v4u g = __builtin_amdgcn_raw_buffer_load_b128(rws, po, 0, AUX_SC1);
        if (__all(lane >= 32 || has_tag(g, tag))) break;
      }
      __builtin_amdgcn_raw_buffer_store_b32(tag, rws, lane == 0 ? WS_EP + b * 128 : OOB, 0, AUX_SC1);
    }
    barrier();
    {
      const v4u oa0 = *(LAS v4u*)ap, oa1 = *(LAS v4u*)(ap + 128);
      SB();
#pragma unroll
      for (int tt = 0; tt < 4; ++tt) accb[tt] = smfma(oa0, ww[tt], accb[tt], sidx);
#pragma unroll
      for (int tt = 0; tt < 4; ++tt) accb[tt] = smfma(oa1, ww[4 + tt], accb[tt], sidx);
    }
    {
      float sm[4];
#pragma unroll
      for (int tt = 0; tt < 4; ++tt) { sm[tt] = accb[tt].x + accb[tt].y; float t2 = accb[tt].z + accb[tt].w; asm volatile("" : "+v"(sm[tt]), "+v"(t2)); sm[tt] += t2; }
      auto s02 = __builtin_amdgcn_permlane32_swap(__float_as_uint(sm[0]), __float_as_uint(sm[2]), false, false);
      auto s13 = __builtin_amdgcn_permlane32_swap(__float_as_uint(sm[1]), __float_as_uint(sm[3]), false, false);
      auto sw = __builtin_amdgcn_permlane16_swap(__float_as_uint(__uint_as_float(s02[0]) + __uint_as_float(s02[1])), __float_as_uint(__uint_as_float(s13[0]) + __uint_as_float(s13[1])), false, false);
      __builtin_amdgcn_raw_buffer_store_b128(tag4(__uint_as_float(sw[0]) + __uint_as_float(sw[1]), tb), rws, fo, 0, AUX_SC1);
    }
    if (w == 0) {
      v4f y4 = {0.f, 0.f, 0.f, 0.f};
      int o0 = io, o1 = io;
      __builtin_amdgcn_s_sleep(PD);
#pragma unroll 1
      for (unsigned spins = 0; spins < SPIN_CAP; ++spins) {
        asm volatile("" : "+v"(o0), "+v"(o1)::"memory");
        const v4u g0 = __builtin_amdgcn_raw_buffer_load_b128(rws, o0, 0, AUX_SC1), g1 = __builtin_amdgcn_raw_buffer_load_b128(rws, o1 + 1024, 0, AUX_SC1);
        const bool got0 = o0 < OOB && has_tag(g0, tag), got1 = o1 < OOB && has_tag(g1, tag);
        y4 += got0 ? untag(g0) : v4f{0.f, 0.f, 0.f, 0.f};
        y4 += got1 ? untag(g1) : v4f{0.f, 0.f, 0.f, 0.f};
        o0 = got0 ? OOB : o0; o1 = got1 ? OOB : o1;
        if (__all(o0 >= OOB && o1 >= OOB)) break;
      }
#pragma unroll
      for (int e = 0; e < 4; ++e) { float t = y4[e]; t += dppf<XOR8>(t); t += dppf<ROR4>(t); y4[e] = rows_sum(t); }
      __builtin_amdgcn_raw_buffer_store_b64(v2u{pk_bf16(y4.x, y4.y), pk_bf16(y4.z, y4.w)}, rout, oo, 0, 0);
    }
    __builtin_amdgcn_endpgm();
  } else {
    Stream S;
    S.rk = mk_rsrc((void*)(ka[1] + kvbase), WIN * 256); S.rv = mk_rsrc((void*)(ka[2] + kvbase), WIN * 256); S.rws = mk_rsrc((void*)ka[6], WS_BYTES);
    S.voff = lane * 16 + w * 1024; S.so = R0 * 8192; S.lane = lane; S.L = L; S.w = w; S.tpo = ((j & 3) * 64 + b * 8 + xcd) * 16;
    S.acc = v4f{0.f, 0.f, 0.f, 0.f};
#pragma unroll
    for (int d = 0; d < 4; ++d) S.msk[d] = (c >> 1) == d ? ((c & 1) ? 0xffff0000u : 0x0000ffffu) : 0u;
    stream_step<R0>(S);
    LAS unsigned* op = (LAS unsigned*)(L + L_OP + w * OPP) + 8 * (c >> 1) + ((2 * c + r) & 3);
    asm volatile("" : "+v"(op));
    eat_vs<v_done(63), 32>(S);
    if (lane < 32) {
      op[0] = pk_bf16(S.acc.x * S.fac, S.acc.y * S.fac);
      op[4] = pk_bf16(S.acc.z * S.fac, S.acc.w * S.fac);
    }
    barrier();
    asm volatile("s_endpgm\n\t.p2align 12");
    __builtin_unreachable();
  }
}

}

extern "C" void kernel_launch(void* const* d_in, const int* in_sizes, int n_in, void* d_out, int out_size, void* d_ws, size_t ws_size,
                              hipStream_t stream) {
  fused_kernel<<<256, 1024, 0, stream>>>(d_in[0], d_in[1], d_in[2], d_in[3], d_in[6], d_in[7], d_ws, d_in[4], d_in[8], d_in[9], d_out);
}
```

```cpp
#include <hip/hip_runtime.h>
#include <hip/hip_bf16.h>

namespace {

typedef unsigned v4u __attribute__((ext_vector_type(4)));
typedef unsigned v2u __attribute__((ext_vector_type(2)));
typedef unsigned v8u __attribute__((ext_vector_type(8)));
typedef __bf16 v16bf __attribute__((ext_vector_type(16)));
typedef float v4f __attribute__((ext_vector_type(4)));
typedef float v2f __attribute__((ext_vector_type(2)));
typedef short v4s __attribute__((ext_vector_type(4)));
typedef __bf16 v8bf __attribute__((ext_vector_type(8)));
typedef __bf16 v2bf __attribute__((ext_vector_type(2)));
typedef unsigned long long v8ul __attribute__((ext_vector_type(8)));
typedef unsigned long long v2ul __attribute__((ext_vector_type(2)));
#define LAS __attribute__((address_space(3)))
typedef __amdgpu_buffer_rsrc_t rsrc_t;

constexpr int DM = 512, NH = 32, SK = 4096, WIN = 1024, FF = 1376;
constexpr int NCOL = 4096 + 2 * FF;
constexpr int WS_EP = 0, WS_PX = 4096, PX_HALF = 8 * NCOL * 4, WS_TP = 480 * 1024, WS_AK = 488 * 1024, WS_FX = 512 * 1024, WS_BYTES = WS_FX + 8 * 32 * 32 * 16 * 4;
constexpr int AUX_NT = 2, AUX_SC1 = 16, OOB = 0x40000000;
constexpr unsigned TAG_STEP = 0x01010101u;
constexpr unsigned SPIN_CAP = 1u << 16;
constexpr int R0 = 2;
constexpr int R = 3;
constexpr int RHI = 5, THI = 25;
constexpr int RLO = 2, TLO = 30;
constexpr int NP = 20, DR = 7;
constexpr int TA = 20, DT = 498, DT2 = 474, NB = 44;
constexpr int N0 = 20;
constexpr int NF = 52;
constexpr int NQ = 2, NG = 8;
constexpr int PD = 16;
constexpr int L_STG = 0;
constexpr int L_K0 = 65536;
constexpr int L_RED = L_K0 + 8 * R0 * 1024;
constexpr int L_X = L_RED + 16384;
constexpr int OPP = 320;
constexpr int L_OP = L_X + 8192;
constexpr int L_EP = L_OP + 8 * OPP;
constexpr int L_Q = L_EP + 1024;
constexpr int L_H = L_Q + 256;
constexpr int L_ST = L_H + 128;
constexpr int L_G = L_ST + 64;
constexpr int L_FL = L_G + 16;
constexpr int L_BYTES = L_FL + 32;

__device__ __forceinline__ rsrc_t mk_rsrc(const void* p, int bytes) { return __builtin_amdgcn_make_buffer_rsrc((void*)p, 0, bytes, 0x00020000); }
__device__ __forceinline__ v4u mk_desc(unsigned long long a, int bytes) { return v4u{(unsigned)a, (unsigned)(a >> 32) & 0xffffu, (unsigned)bytes, 0x00020000u}; }
__device__ __forceinline__ unsigned pk_bf16(float lo, float hi) { v2bf r = {(__bf16)lo, (__bf16)hi}; return __builtin_bit_cast(unsigned, r); }
__device__ __forceinline__ float rbf(float x) { return (float)(__bf16)x; }
__device__ __forceinline__ float bf_lo(unsigned u) { return __builtin_bit_cast(float, u << 16); }
__device__ __forceinline__ float bf_hi(unsigned u) { return __builtin_bit_cast(float, u & 0xffff0000u); }
__device__ __forceinline__ float dot2(unsigned a, unsigned b, float c) { return __builtin_amdgcn_fdot2_f32_bf16(__builtin_bit_cast(v2bf, a), __builtin_bit_cast(v2bf, b), c, false); }
__device__ __forceinline__ v4f mfma(v4u a, v4u b, v4f c) { return __builtin_amdgcn_mfma_f32_16x16x32_bf16(__builtin_bit_cast(v8bf, a), __builtin_bit_cast(v8bf, b), c, 0, 0, 0); }
__device__ __forceinline__ v4f smfma(v4u a, v8u b, v4f c, int idx) { return __builtin_amdgcn_smfmac_f32_16x16x64_bf16(__builtin_bit_cast(v8bf, a), __builtin_bit_cast(v16bf, b), c, idx, 0, 0); }
template <int CTRL> __device__ __forceinline__ unsigned dppu(unsigned x) { return (unsigned)__builtin_amdgcn_mov_dpp((int)x, CTRL, 0xf, 0xf, true); }
template <int CTRL> __device__ __forceinline__ float dppf(float x) { return __builtin_bit_cast(float, dppu<CTRL>(__builtin_bit_cast(unsigned, x))); }
constexpr int XOR1 = 0xB1, XOR2 = 0x4E, XOR7 = 0x141, XOR8 = 0x128, ROR4 = 0x124, BCAST = 0x150;
__device__ __forceinline__ float swap32_sum(float x) {
  auto t = __builtin_amdgcn_permlane32_swap(__float_as_uint(x), __float_as_uint(x), false, false);
  return __uint_as_float(t[0]) + __uint_as_float(t[1]);
}
__device__ __forceinline__ float rows_sum(float x) {
  auto s = __builtin_amdgcn_permlane16_swap(__float_as_uint(x), __float_as_uint(x), false, false);
  return swap32_sum(__uint_as_float(s[0]) + __uint_as_float(s[1]));
}
__device__ __forceinline__ float rows_max(float x) {
  auto s = __builtin_amdgcn_permlane16_swap(__float_as_uint(x), __float_as_uint(x), false, false);
  x = fmaxf(__uint_as_float(s[0]), __uint_as_float(s[1]));
  auto t = __builtin_amdgcn_permlane32_swap(__float_as_uint(x), __float_as_uint(x), false, false);
  return fmaxf(__uint_as_float(t[0]), __uint_as_float(t[1]));
}
__device__ __forceinline__ float wave_sum(float x) { x += dppf<XOR8>(x); x += dppf<XOR7>(x); x += dppf<XOR2>(x); x += dppf<XOR1>(x); return rows_sum(x); }
__device__ __forceinline__ float wave_max(float x) { x = fmaxf(x, dppf<XOR8>(x)); x = fmaxf(x, dppf<XOR7>(x)); x = fmaxf(x, dppf<XOR2>(x)); x = fmaxf(x, dppf<XOR1>(x)); return rows_max(x); }
template <int N> __device__ __forceinline__ void wait_vm() { asm volatile("s_waitcnt vmcnt(%0)" ::"i"(N) : "memory"); }
__device__ __forceinline__ void barrier() { asm volatile("s_waitcnt lgkmcnt(0)\n\ts_barrier" ::: "memory"); }
__device__ __forceinline__ unsigned lds_peek(LAS unsigned* p) { return __builtin_amdgcn_readfirstlane(__hip_atomic_load(p, __ATOMIC_RELAXED, __HIP_MEMORY_SCOPE_WORKGROUP)); }
__device__ __forceinline__ void lds_wait_ge(LAS unsigned* p, unsigned want) {
  while (lds_peek(p) < want) __builtin_amdgcn_s_sleep(2);
  asm volatile("" ::: "memory");
}
__device__ __forceinline__ void lds_signal(LAS unsigned* p, int lane) {
  asm volatile("s_waitcnt lgkmcnt(0)" ::: "memory");
  if (lane == 0) __hip_atomic_fetch_add(p, 1u, __ATOMIC_RELAXED, __HIP_MEMORY_SCOPE_WORKGROUP);
  asm volatile("" ::: "memory");
}
__device__ __forceinline__ v4u tag_bytes(unsigned tag) { return v4u{tag & 0xffu, (tag >> 8) & 0xffu, (tag >> 16) & 0xffu, tag >> 24}; }
__device__ __forceinline__ v4u tag4(float y, v4u tb) {
  const unsigned u = (__float_as_uint(y) + 0x80u) & 0xffffff00u;
  return v4u{u | tb.x, dppu<0x55>(u) | tb.y, dppu<0xAA>(u) | tb.z, dppu<0xFF>(u) | tb.w};
}
__device__ __forceinline__ bool has_tag(v4u g, unsigned tag) {
  return __builtin_amdgcn_perm(__builtin_amdgcn_perm(g.w, g.z, 0x0c0c0400u), __builtin_amdgcn_perm(g.y, g.x, 0x0c0c0400u), 0x05040100u) == tag;
}
__device__ __forceinline__ v4f untag(v4u g) { return __builtin_bit_cast(v4f, g & 0xffffff00u); }
#define STUB(n) ".p2align 12\n.Lgm_page" #n ":\n\ts_setpc_b64 s[60:61]\n"
#define PAGE(n) asm volatile("s_branch .Lgm_after" #n "\n\t" STUB(n) ".Lgm_after" #n ":" ::: "memory")
__device__ __forceinline__ void visit(int which) {
  asm volatile("s_cmp_eq_u32 %0, 13\n\ts_cselect_b32 s60, .Lgm_page1-.Lgm_from, 0\n\ts_cmp_eq_u32 %0, 14\n\ts_cselect_b32 s60, .Lgm_page2-.Lgm_from, s60\n\t"
               "s_cmp_eq_u32 %0, 15\n\ts_cselect_b32 s60, .Lgm_page3-.Lgm_from, s60\n\ts_getpc_b64 s[62:63]\n.Lgm_from:\n\t"
               "s_add_u32 s62, s62, s60\n\ts_addc_u32 s63, s63, 0\n\ts_swappc_b64 s[60:61], s[62:63]" ::"s"(which) : "s60", "s61", "s62", "s63", "scc", "memory");
}
#define SB() __builtin_amdgcn_sched_barrier(0)
#define PIN_S(x) asm volatile("" : "+s"(x))
constexpr int rev4(int i) { return ((i & 1) << 3) | ((i & 2) << 1) | ((i & 4) >> 1) | ((i & 8) >> 3); }

struct Stream {
  rsrc_t rk, rv, rws;
  int voff, so, lane, w, tpo, lag;
  LAS unsigned char* L;
  v4u a[64], q, tp;
  float lv[4], s[2], m, fac;
  unsigned pp[2], msk[4], tag, tp0;
  v4f acc;
};
constexpr int k_done(int n) { int d = n < N0 ? 0 : 2 * (n - N0 + 1); int lim = n - RHI + 1; d = d < lim ? d : lim; return d < 0 ? 0 : (d > 32 ? 32 : d); }
constexpr int soft_step() { int n = 0; while (k_done(n) < 32) ++n; return n + 1; }
constexpr int v_done(int n) { int d = n <= soft_step() ? 0 : 2 * (n - soft_step()); int lim = n - RHI + 1 - 32; d = d < lim ? d : lim; return d < 0 ? 0 : (d > 32 ? 32 : d); }
constexpr int sched(int n) { return ((n < NB ? n : NB) * DT + (n < NB ? 0 : n - NB) * DT2) / 16; }
constexpr int NC = NP + DR + RHI + 1;

template <int I> __device__ __forceinline__ void eat_k(Stream& S) {
  if constexpr (I < R0) S.a[I] = *(LAS v4u*)(S.L + L_K0 + (S.w * R0 + I) * 1024 + S.lane * 16);
  float d = dot2(S.a[I].x, S.q.x, 0.f);
  d = dot2(S.a[I].y, S.q.y, d); d = dot2(S.a[I].z, S.q.z, d); d = dot2(S.a[I].w, S.q.w, d);
  const int c = S.lane;
#define TR_LEVEL(LV, CTRL, BIT) \
  if constexpr ((I & (1 << LV)) == 0) { S.lv[LV] = d; return; } \
  { const float t1 = S.lv[LV] + dppf<CTRL>(S.lv[LV]), t2 = d + dppf<CTRL>(d); d = (c & BIT) ? t2 : t1; }
  TR_LEVEL(0, XOR8, 8) TR_LEVEL(1, XOR7, 4) TR_LEVEL(2, XOR2, 2) TR_LEVEL(3, XOR1, 1)
#undef TR_LEVEL
  S.s[I >> 4] = d;
}
template <int I> __device__ __forceinline__ void eat_v(Stream& S) {
  const unsigned t = dppu<BCAST + rev4(I & 15)>(S.pp[I >> 4]);
  const v4u cf = {t & S.msk[0], t & S.msk[1], t & S.msk[2], t & S.msk[3]};
  S.acc = mfma(cf, S.a[32 + I], S.acc);
}
template <int LO, int HI> __device__ __forceinline__ void eat_ks(Stream& S) { if constexpr (LO < HI) { eat_k<LO>(S); eat_ks<LO + 1, HI>(S); } }
template <int LO, int HI> __device__ __forceinline__ void eat_vs(Stream& S) { if constexpr (LO < HI) { eat_v<LO>(S); SB(); eat_vs<LO + 1, HI>(S); } }

template <int N> __device__ __forceinline__ void stream_step(Stream& S) {
  if constexpr (N < 64) {
    LAS unsigned* fl = (LAS unsigned*)(S.L + L_FL);
    if constexpr (N == 37) PAGE(1);
    if constexpr (N >= NC) {
      if constexpr ((N - NC) % 3 == 0) {
        const unsigned org = lds_peek(fl + 4);
        const int lag = (int)((unsigned)__builtin_amdgcn_s_memrealtime() - org) - (TA + sched(N));
        S.lag = org ? lag : -1;
      }
      asm volatile("s_cmpk_gt_i32 %0, %1\n\ts_cbranch_scc1 1f\n\ts_cmpk_gt_i32 %0, %2\n\ts_cbranch_scc1 2f\n\t"
                   "s_waitcnt vmcnt(%3)\n2:\n\ts_waitcnt vmcnt(%4)\n1:\n\ts_waitcnt vmcnt(%5)"
                   ::"s"(S.lag), "i"(THI), "i"(-TLO & 0xffff), "i"(RLO - 1), "i"(R - 1), "i"(RHI - 1) : "memory", "scc");
    } else if constexpr (N >= R) wait_vm<R - 1>();
    if constexpr (N == 32) S.so = 0;
    S.a[N] = __builtin_amdgcn_raw_buffer_load_b128(N < 32 ? S.rk : S.rv, S.voff, S.so, AUX_NT);
    SB();
    if constexpr (N != 31 && N != 63) asm volatile("s_addk_i32 %0, 0x2000" : "+s"(S.so)::"scc");
    {
      if constexpr (N == NP) {
        if (S.w == 0) {
          S.tp0 = (unsigned)__builtin_amdgcn_s_memrealtime();
          lds_wait_ge(fl + 0, 8u);
          S.tag = *(LAS unsigned*)(S.L + L_EP + (S.lane >> 3) * 16) + TAG_STEP;
          __builtin_amdgcn_raw_buffer_store_b128(tag_bytes(S.tag) | (S.tp0 << 8), S.rws, S.lane == ((S.tpo >> 4) & 63) ? WS_TP + S.tpo : OOB, 0, AUX_SC1);
        }
      }
      if constexpr (N == NP + DR) { if (S.w == 0) S.tp = __builtin_amdgcn_raw_buffer_load_b128(S.rws, WS_TP + (S.tpo & ~1023) + S.lane * 16, 0, AUX_SC1); }
      if constexpr (N == NC - 1) {
        if (S.w == 0) {
          const bool ok = has_tag(S.tp, S.tag);
          const float dsum = wave_sum(ok ? (float)((int)(S.tp.x - (S.tp0 << 8)) >> 8) : 0.f), cnt = (float)__builtin_popcountll(__ballot(ok));
          const unsigned org = (S.tp0 + (int)(dsum * __builtin_amdgcn_rcpf(fmaxf(cnt, 1.f))) - sched(NP)) | 1u;
          if (S.lane == 0) __hip_atomic_store(fl + 4, org, __ATOMIC_RELAXED, __HIP_MEMORY_SCOPE_WORKGROUP);
        }
      }
    }
    if constexpr (N == N0) {
      lds_wait_ge(fl + 1, 1u);
      S.q = *(LAS v4u*)(S.L + L_Q + (S.lane & 15) * 16);
    }
    eat_ks<k_done(N - 1), k_done(N)>(S);
    if constexpr (N == soft_step()) {
      constexpr float C2 = 0.08838834764831845f * 1.4426950408889634f;
      const float s0 = S.s[0] * C2, s1 = S.s[1] * C2;
      S.m = wave_max(fmaxf(s0, s1));
      const float p0 = __builtin_amdgcn_exp2f(s0 - S.m), p1 = __builtin_amdgcn_exp2f(s1 - S.m);
      S.pp[0] = pk_bf16(p0, p0); S.pp[1] = pk_bf16(p1, p1);
      const float l = wave_sum(bf_lo(S.pp[0]) + bf_lo(S.pp[1]));
      if (S.lane == 0) *(LAS v2f*)(S.L + L_ST + S.w * 8) = v2f{S.m, l};
      lds_signal(fl + 3, S.lane);
    }
    if constexpr (N == NF) {
      lds_wait_ge(fl + 3, 8u);
      lds_wait_ge(fl + 2, 1u);
      const v2f st = *(LAS v2f*)(S.L + L_ST + (S.lane & 7) * 8);
      float M = fmaxf(st.x, dppf<XOR7>(st.x)); M = fmaxf(M, dppf<XOR2>(M)); M = fmaxf(M, dppf<XOR1>(M));
      float Ls = __builtin_amdgcn_exp2f(st.x - M) * st.y; Ls += dppf<XOR7>(Ls); Ls += dppf<XOR2>(Ls); Ls += dppf<XOR1>(Ls);
      S.fac = __builtin_amdgcn_exp2f(S.m - M) * *(LAS float*)(S.L + L_G) / Ls;
      asm volatile("" : "+v"(S.fac));
    }
    eat_vs<v_done(N - 1), v_done(N)>(S);
    SB();
    stream_step<N + 1>(S);
  }
}

__device__ __forceinline__ void to_frags(LAS unsigned char* tr, v4u* fr) {
#pragma unroll
  for (int t = 0; t < 4; ++t) {
    const v2u lo = __builtin_bit_cast(v2u, __builtin_amdgcn_ds_read_tr16_b64_v4i16((LAS v4s*)(tr + t * 32)));
    const v2u hi = __builtin_bit_cast(v2u, __builtin_amdgcn_ds_read_tr16_b64_v4i16((LAS v4s*)(tr + t * 32 + 512)));
    fr[t] = v4u{lo.x, lo.y, hi.x, hi.y};
  }
  asm volatile("" : "+v"(fr[0]), "+v"(fr[1]), "+v"(fr[2]), "+v"(fr[3])::"memory");
}
template <int I, int AUX> __device__ __forceinline__ void dma(int m0v, int voff, v4u desc, int soff) {
#define DMA(POL) asm volatile("s_mov_b32 m0, %0\n\ts_nop 0\n\tbuffer_load_dwordx4 %1, %2, %3 offen offset:%4 " POL " lds" ::"s"(m0v), "v"(voff), "s"(desc), "s"(soff - I * 1024), "i"(I * 1024) : "memory")
  if constexpr (AUX == AUX_NT) DMA("nt"); else if constexpr (AUX == AUX_SC1) DMA("sc1"); else DMA("");
#undef DMA
}
template <int N, class FQ, class FH> __device__ __forceinline__ void weight_step(int stg, LAS unsigned char* tr, int voff, v4u d2, v4u dO, v4u* fr, LAS unsigned* late, FQ& gather_q, FH& gather_h) {
  if constexpr (N < 22) {
    if constexpr (N == 13) PAGE(3);
    if constexpr (N == NQ) gather_q();
    if constexpr (N == NG) gather_h();
    if constexpr (N % 4 == 0) {
      wait_vm<0>();
      if constexpr (N > 0 && N <= 16) to_frags(tr + ((N / 4) & 1) * 4096, fr + N - 4);
    } else if constexpr (N % 4 == 2) {
      if (N < 8 || lds_peek(late) == 0u) wait_vm<0>();
    }
    dma<N % 4, 0>(stg + ((N / 4 + 1) & 1) * 4096, voff, N < 16 ? dO : d2, (N < 16 ? N : N - 16) * 8192);
    SB();
    weight_step<N + 1>(stg, tr, voff, d2, dO, fr, late, gather_q, gather_h);
  }
}

__global__ void __launch_bounds__(1024, 4) __attribute__((aligned(4096)))
fused_kernel(const void* x_, const void* kc_, const void* vc_, const void* wq_, const void* w1_, const void* w3_, void* ws_, const void* wo_,
             const void* w2_, const void* gw_, void* out_) {
  __shared__ __attribute__((aligned(16))) unsigned char smem[L_BYTES];
  LAS unsigned char* L = (LAS unsigned char*)smem;
  v8ul ka;
  v2ul kb;
  unsigned long long outp;
  asm volatile("s_load_dwordx16 %0, %3, 0x0\n\ts_load_dwordx4 %1, %3, 0x40\n\ts_load_dwordx2 %2, %3, 0x50" : "=&s"(ka), "=&s"(kb), "=&s"(outp) : "s"(__builtin_amdgcn_kernarg_segment_ptr()));
  SB();
  const int tid = threadIdx.x, lane = tid & 63, w16 = __builtin_amdgcn_readfirstlane(tid >> 6), w = w16 & 7;
  if (w16 >= 13) visit(w16);
  const int bid = blockIdx.x, xcd = bid & 7, j = bid >> 3, h = 4 * xcd + (j & 3), b = j >> 2;
  const int r = lane >> 4, c = lane & 15;
  const bool streamer = w16 >= 8;
  const bool isq = j < 16, valid = j < 27;
  const int qm = (j - 16) >> 31;
  const int un = (11 + (qm & 5)) * xcd + (j & 15), par = un & 1, mat = (un >> 1) & 1, sl = un >> (2 + qm);
  const int ustr = 2 * FF + (qm & (8192 - 2 * FF));
  const int col0 = 64 * sl - (~qm & 32 * par);
  const int c0 = h * 43 / 8, nch = (h + 1) * 43 / 8 - c0;
  const size_t kvbase = ((size_t)(b * NH + h) * SK + (SK - WIN)) * 256;
  const int cb = col0 * 2 + (lane & 7) * 16;
  int vu = (2 * (32 * w + (lane >> 3)) + par) * ustr + cb;
  asm volatile("" : "+v"(vu));
  vu = (unsigned)cb < (unsigned)ustr ? vu : OOB;
  int vo0 = lane * 16 + w * 1024;
  asm volatile("" : "+v"(vu), "+v"(vo0));
  vo0 = streamer ? vo0 : vu;
  const int nb0 = streamer ? WIN * 256 : (valid ? DM * ustr : 0), st0 = streamer ? 8192 : 16 * ustr;
  const int m00 = streamer ? L_K0 + w * R0 * 1024 : L_STG + w * 8192;
  asm volatile("s_waitcnt lgkmcnt(0)" : "+s"(ka), "+s"(kb), "+s"(outp), "+v"(vo0) : "s"(nb0), "s"(st0), "s"(m00), "s"(kvbase));
  {
    unsigned long long p0 = mat ? ka[5] : ka[4], pk = ka[1] + kvbase;
    asm volatile("" : "+s"(p0), "+s"(pk));
    p0 = isq ? ka[3] : p0;
    PIN_S(p0);
    const v4u d0 = mk_desc(streamer ? pk : p0, nb0);
    dma<0, AUX_NT>(m00, vo0, d0, 0); dma<1, AUX_NT>(m00, vo0, d0, st0);
    SB();
    if (!streamer) {
      dma<2, AUX_NT>(m00, vo0, d0, 2 * st0); dma<3, AUX_NT>(m00, vo0, d0, 3 * st0);
      dma<0, 0>(L_X + w * 1024, (lane >> 3) * (DM * 2) + (lane & 7) * 16, mk_desc(ka[0], 8 * DM * 2), w * 128);
      unsigned long long tp = ka[6], t1 = ka[2] + kvbase, t2 = ka[7] + h * 131072, t3 = kb[0] + c0 * 8192, t5 = outp + (b * DM + 16 * h) * 2;
      int tn = w == 7 ? 1024 : 0;
      asm volatile("" : "+s"(t1), "+s"(t2), "+s"(t3), "+s"(t5), "+s"(tn));
      tp = w == 1 ? t1 : tp; PIN_S(tp);
      tp = w == 2 ? t2 : tp; PIN_S(tp);
      tp = w == 3 ? t3 : tp; PIN_S(tp);
      tp = w == 4 ? kb[1] : tp; PIN_S(tp);
      tp = w == 5 ? t5 : tp;
      tn = (unsigned)(w - 1) < 5u ? 16 : tn;
      dma<0, AUX_SC1>(w == 7 ? L_EP : L_RED + w * 2048, lane < 8 ? lane * 128 : OOB, mk_desc(tp, tn), 0);
    }
  }
  if (tid < 8) ((LAS unsigned*)(L + L_FL))[tid] = 0u;
  barrier();
  int role = w16 >> 3;
  PIN_S(role);
  if (role == 0) {
    LAS unsigned* fl = (LAS unsigned*)(L + L_FL);
    const rsrc_t rws = mk_rsrc((void*)ka[6], WS_BYTES);
    LAS unsigned char* tr = L + L_STG + w * 8192 + (8 * r + (c >> 2)) * 128 + (c & 3) * 8;
    {
      v4u uf[4];
      wait_vm<0>();
      to_frags(tr, uf);
      const unsigned ps = par ? 0x07060302u : 0x05040100u;
      const v4u x0 = *(LAS v4u*)(L + L_X + w * 1024 + (c & 7) * 128 + r * 32), x1 = *(LAS v4u*)(L + L_X + w * 1024 + (c & 7) * 128 + r * 32 + 16);
      const v4u xa = {__builtin_amdgcn_perm(x0.y, x0.x, ps), __builtin_amdgcn_perm(x0.w, x0.z, ps), __builtin_amdgcn_perm(x1.y, x1.x, ps), __builtin_amdgcn_perm(x1.w, x1.z, ps)};
#pragma unroll
      for (int tt = 0; tt < 4; ++tt) {
        const v4f ua = mfma(xa, uf[tt], v4f{0.f, 0.f, 0.f, 0.f});
        if (lane < 32) *(LAS v4f*)(L + L_RED + w * 2048 + tt * 512 + lane * 16) = ua;
      }
      lds_signal(fl + 0, lane);
      lds_wait_ge(fl + 0, 8u);
      const int ro = L_RED + r * 512 + ((w >> 2) * 16 + c) * 16 + (w & 3) * 4;
      float sum = 0.f;
#pragma unroll
      for (int ww = 0; ww < 8; ++ww) sum += *(LAS float*)(L + ro + ww * 2048);
      const int col = col0 + lane;
      const bool on = valid && !(lane & 3) && (isq || (unsigned)col < (unsigned)FF);
      __builtin_amdgcn_raw_buffer_store_b128(tag4(sum, tag_bytes(*(LAS unsigned*)(L + L_EP + w * 16) + TAG_STEP)), rws,
                                             on ? WS_PX + par * PX_HALF + (w * NCOL + (isq ? 0 : 4096 + mat * FF) + col) * 4 : OOB, 0, AUX_SC1);
    }
    const unsigned tag = *(LAS unsigned*)(L + L_EP + b * 16) + TAG_STEP;
    auto gather_q = [&]() {
      if (w == 0) {
        v4u g;
        int po = WS_PX + (lane >> 5) * PX_HALF + (b * NCOL + h * 128 + 4 * (lane & 31)) * 4;
#pragma unroll 1
        for (unsigned spins = 0; spins < SPIN_CAP; ++spins) {
          asm volatile("" : "+v"(po)::"memory");
          g = __builtin_amdgcn_raw_buffer_load_b128(rws, po, 0, AUX_SC1);
          if (__all(has_tag(g, tag))) break;
        }
        const v4f v = untag(g);
        const v2u qq = {pk_bf16(swap32_sum(v.x), swap32_sum(v.y)), pk_bf16(swap32_sum(v.z), swap32_sum(v.w))};
        if (lane < 32) *(LAS v2u*)(L + L_Q + lane * 8) = qq;
        lds_signal(fl + 1, lane);
      }
    };
    auto gather_h = [&]() {
      if (w == 1) {
        const v4u gx = __builtin_amdgcn_raw_buffer_load_b128(mk_rsrc((void*)ka[0], 8 * DM * 2), b * DM * 2 + lane * 16, 0, 0);
        const v4u gg = __builtin_amdgcn_raw_buffer_load_b128(mk_rsrc((void*)kb[1], DM * 2), lane * 16, 0, 0);
        const bool on = c < 2 * nch;
        int po = on ? WS_PX + (r >> 1) * PX_HALF + (b * NCOL + 4096 + (r & 1) * FF + 8 * c0 + 4 * c) * 4 : OOB;
        v4u gu;
#pragma unroll 1
        for (unsigned spins = 0; spins < SPIN_CAP; ++spins) {
          asm volatile("" : "+v"(po)::"memory");
          gu = __builtin_amdgcn_raw_buffer_load_b128(rws, po, 0, AUX_SC1);
          if (__all(!on || has_tag(gu, tag))) break;
        }
        float s = 0.f;
#pragma unroll
        for (int d = 0; d < 4; ++d) s += rbf(bf_lo(gx[d]) * bf_lo(gg[d])) + rbf(bf_hi(gx[d]) * bf_hi(gg[d]));
        s = rbf(wave_sum(s));
        const float g = rbf(1.0f / (1.0f + __expf(-s)));
        if (lane == 0) *(LAS v2f*)(L + L_G) = v2f{g, rbf(1.0f - g)};
        const v4f pv = untag(gu);
        float hv[4];
#pragma unroll
        for (int e = 0; e < 4; ++e) {
          const float t = rbf(swap32_sum(pv[e]));
          auto sw = __builtin_amdgcn_permlane16_swap(__float_as_uint(t), __float_as_uint(t), false, false);
          const float gv = __uint_as_float(sw[0]), uv = __uint_as_float(sw[1]);
          hv[e] = rbf(gv / (1.0f + __expf(-gv))) * uv;
        }
        if (lane < 16) *(LAS v2u*)(L + L_H + lane * 8) = v2u{pk_bf16(hv[0], hv[1]), pk_bf16(hv[2], hv[3])};
        lds_signal(fl + 2, lane);
        lds_wait_ge(fl + 1, 1u);
        __builtin_amdgcn_raw_buffer_store_b128(tag_bytes(tag), rws, lane == 0 ? WS_AK + (b * 32 + h) * 16 : OOB, 0, AUX_SC1);
      }
    };
    v4u wf[16];
    weight_step<0>(L_STG + w * 8192, tr, (lane >> 3) * 1024 + w * 128 + (lane & 7) * 16, mk_desc(kb[0] + c0 * 8192, nch * 8192), mk_desc(ka[7] + h * 131072, 131072),
                   wf, fl + 3, gather_q, gather_h);
    wait_vm<0>();
    v4f accb[4];
    {
      lds_wait_ge(fl + 2, 1u);
      v4u h0 = *(LAS v4u*)(L + L_H + r * 16), h1 = *(LAS v4u*)(L + L_H + 64 + r * 16);
      if (c) h0 = h1 = v4u{0u, 0u, 0u, 0u};
      const float omg = *(LAS float*)(L + L_G + 4);
      v4u f2[4];
      to_frags(tr + 4096, f2);
#pragma unroll
      for (int tt = 0; tt < 4; ++tt) accb[tt] = mfma(h0, f2[tt], v4f{0.f, 0.f, 0.f, 0.f});
      to_frags(tr, f2);
#pragma unroll
      for (int tt = 0; tt < 4; ++tt) accb[tt] = mfma(h1, f2[tt], accb[tt]) * omg;
    }
    int fo = (lane & 3) ? OOB : WS_FX + ((b * 32 + 4 * w + r) * 32 + h) * 64 + c * 4;
    int io = WS_FX + (b * 32 + h) * 2048 + lane * 16;
    int oo = lane < 4 ? (b * DM + 16 * h + 4 * lane) * 2 : OOB;
    const rsrc_t rout = mk_rsrc((void*)outp, 8 * DM * 2);
    LAS unsigned char* ap = L + L_OP + (c & 7) * OPP + r * 32 + (c >> 3) * 16;
    int sidx = (c >> 3) ? 0xEEEE : 0x4444;
    v8u ww[8];
#pragma unroll
    for (int i = 0; i < 8; ++i) ww[i] = __builtin_shufflevector(wf[(i >> 2) * 8 + (i & 3)], wf[(i >> 2) * 8 + 4 + (i & 3)], 0, 1, 2, 3, 4, 5, 6, 7);
    asm volatile("" : "+v"(ww[0]), "+v"(ww[1]), "+v"(ww[2]), "+v"(ww[3]), "+v"(ww[4]), "+v"(ww[5]), "+v"(ww[6]), "+v"(ww[7]), "+v"(sidx));
    v4u tb = tag_bytes(tag);
    asm volatile("" : "+v"(fo), "+v"(io), "+v"(oo), "+v"(tb), "+v"(ap));
    asm volatile("" : "+v"(accb[0]), "+v"(accb[1]), "+v"(accb[2]), "+v"(accb[3]));
    if (w == 0) {
      const v4u t0 = __builtin_amdgcn_raw_buffer_load_b128(rws, io, 0, AUX_SC1), t1 = __builtin_amdgcn_raw_buffer_load_b128(rws, io + 1024, 0, AUX_SC1);
      asm volatile("" ::"v"(t0), "v"(t1));
    }
    if (w == 3 && h == 0) {
      int po = lane < 32 ? WS_AK + (b * 32 + lane) * 16 : OOB;
#pragma unroll 1
      for (unsigned spins = 0; spins < SPIN_CAP; ++spins) {
        asm volatile("" : "+v"(po)::"memory");
        const v4u g = __builtin_amdgcn_raw_buffer_load_b128(rws, po, 0, AUX_SC1);
        if (__all(lane >= 32 || has_tag(g, tag))) break;
      }
      __builtin_amdgcn_raw_buffer_store_b32(tag, rws, lane == 0 ? WS_EP + b * 128 : OOB, 0, AUX_SC1);
    }
    barrier();
    {
      const v4u oa0 = *(LAS v4u*)ap, oa1 = *(LAS v4u*)(ap + 128);
      SB();
#pragma unroll
      for (int tt = 0; tt < 4; ++tt) accb[tt] = smfma(oa0, ww[tt], accb[tt], sidx);
#pragma unroll
      for (int tt = 0; tt < 4; ++tt) accb[tt] = smfma(oa1, ww[4 + tt], accb[tt], sidx);
    }
    {
      float sm[4];
#pragma unroll
      for (int tt = 0; tt < 4; ++tt) { sm[tt] = accb[tt].x + accb[tt].y; float t2 = accb[tt].z + accb[tt].w; asm volatile("" : "+v"(sm[tt]), "+v"(t2)); sm[tt] += t2; }
      auto s02 = __builtin_amdgcn_permlane32_swap(__float_as_uint(sm[0]), __float_as_uint(sm[2]), false, false);
      auto s13 = __builtin_amdgcn_permlane32_swap(__float_as_uint(sm[1]), __float_as_uint(sm[3]), false, false);
      auto sw = __builtin_amdgcn_permlane16_swap(__float_as_uint(__uint_as_float(s02[0]) + __uint_as_float(s02[1])), __float_as_uint(__uint_as_float(s13[0]) + __uint_as_float(s13[1])), false, false);
      __builtin_amdgcn_raw_buffer_store_b128(tag4(__uint_as_float(sw[0]) + __uint_as_float(sw[1]), tb), rws, fo, 0, AUX_SC1);
    }
    if (w == 0) {
      v4f y4 = {0.f, 0.f, 0.f, 0.f};
      int o0 = io, o1 = io;
      __builtin_amdgcn_s_sleep(PD);
#pragma unroll 1
      for (unsigned spins = 0; spins < SPIN_CAP; ++spins) {
        asm volatile("" : "+v"(o0), "+v"(o1)::"memory");
        const v4u g0 = __builtin_amdgcn_raw_buffer_load_b128(rws, o0, 0, AUX_SC1), g1 = __builtin_amdgcn_raw_buffer_load_b128(rws, o1 + 1024, 0, AUX_SC1);
        const bool got0 = o0 < OOB && has_tag(g0, tag), got1 = o1 < OOB && has_tag(g1, tag);
        y4 += got0 ? untag(g0) : v4f{0.f, 0.f, 0.f, 0.f};
        y4 += got1 ? untag(g1) : v4f{0.f, 0.f, 0.f, 0.f};
        o0 = got0 ? OOB : o0; o1 = got1 ? OOB : o1;
        if (__all(o0 >= OOB && o1 >= OOB)) break;
      }
#pragma unroll
      for (int e = 0; e < 4; ++e) { float t = y4[e]; t += dppf<XOR8>(t); t += dppf<ROR4>(t); y4[e] = rows_sum(t); }
      __builtin_amdgcn_raw_buffer_store_b64(v2u{pk_bf16(y4.x, y4.y), pk_bf16(y4.z, y4.w)}, rout, oo, 0, 0);
    }
    __builtin_amdgcn_endpgm();
  } else {
    Stream S;
    S.rk = mk_rsrc((void*)(ka[1] + kvbase), WIN * 256); S.rv = mk_rsrc((void*)(ka[2] + kvbase), WIN * 256); S.rws = mk_rsrc((void*)ka[6], WS_BYTES);
    S.voff = lane * 16 + w * 1024; S.so = R0 * 8192; S.lane = lane; S.L = L; S.w = w; S.tpo = ((j & 3) * 64 + b * 8 + xcd) * 16;
    S.acc = v4f{0.f, 0.f, 0.f, 0.f};
#pragma unroll
    for (int d = 0; d < 4; ++d) S.msk[d] = (c >> 1) == d ? ((c & 1) ? 0xffff0000u : 0x0000ffffu) : 0u;
    stream_step<R0>(S);
    LAS unsigned* op = (LAS unsigned*)(L + L_OP + w * OPP) + 8 * (c >> 1) + ((2 * c + r) & 3);
    asm volatile("" : "+v"(op));
    eat_vs<v_done(63), 32>(S);
    if (lane < 32) {
      op[0] = pk_bf16(S.acc.x * S.fac, S.acc.y * S.fac);
      op[4] = pk_bf16(S.acc.z * S.fac, S.acc.w * S.fac);
    }
    barrier();
    asm volatile("s_endpgm\n\t" STUB(2));
    __builtin_unreachable();
  }
}

}

extern "C" void kernel_launch(void* const* d_in, const int* in_sizes, int n_in, void* d_out, int out_size, void* d_ws, size_t ws_size,
                              hipStream_t stream) {
  fused_kernel<<<256, 1024, 0, stream>>>(d_in[0], d_in[1], d_in[2], d_in[3], d_in[6], d_in[7], d_ws, d_in[4], d_in[8], d_in[9], d_out);
}
```

```cpp
#include <hip/hip_runtime.h>
#include <hip/hip_bf16.h>

namespace {

typedef unsigned v4u __attribute__((ext_vector_type(4)));
typedef unsigned v2u __attribute__((ext_vector_type(2)));
typedef unsigned v8u __attribute__((ext_vector_type(8)));
typedef __bf16 v16bf __attribute__((ext_vector_type(16)));
typedef float v4f __attribute__((ext_vector_type(4)));
typedef float v2f __attribute__((ext_vector_type(2)));
typedef short v4s __attribute__((ext_vector_type(4)));
typedef __bf16 v8bf __attribute__((ext_vector_type(8)));
typedef __bf16 v2bf __attribute__((ext_vector_type(2)));
typedef unsigned long long v8ul __attribute__((ext_vector_type(8)));
typedef unsigned long long v2ul __attribute__((ext_vector_type(2)));
#define LAS __attribute__((address_space(3)))
typedef __amdgpu_buffer_rsrc_t rsrc_t;

constexpr int DM = 512, NH = 32, SK = 4096, WIN = 1024, FF = 1376;
constexpr int NCOL = 4096 + 2 * FF;
constexpr int WS_EP = 0, WS_PX = 4096, PX_HALF = 8 * NCOL * 4, WS_TP = 480 * 1024, WS_AK = 488 * 1024, WS_FX = 512 * 1024, WS_BYTES = WS_FX + 8 * 32 * 32 * 16 * 4;
constexpr int AUX_NT = 2, AUX_SC1 = 16, AUX_SC1_NT = 18;
constexpr int OOB = 0x40000000;
constexpr unsigned TAG_STEP = 0x01010101u;
constexpr unsigned SPIN_CAP = 1u << 16;
constexpr int R0 = 2;
constexpr int R = 3;
constexpr int RHI = 5, THI = 25;
constexpr int RLO = 2, TLO = 30;
constexpr int NP = 20, DR = 7;
constexpr int TA = 20, DT = 502, DT2 = 478, NB = 44;
constexpr int N0 = 20;
constexpr int NF = 52;
constexpr int NQ = 2, NG = 8;
constexpr int PD = 16;
constexpr int L_STG = 0;
constexpr int L_K0 = 65536;
constexpr int L_RED = L_K0 + 8 * R0 * 1024;
constexpr int L_X = L_RED + 16384;
constexpr int OPP = 320;
constexpr int L_OP = L_X + 8192;
constexpr int L_EP = L_OP + 8 * OPP;
constexpr int L_Q = L_EP + 1024;
constexpr int L_H = L_Q + 256;
constexpr int L_ST = L_H + 128;
constexpr int L_G = L_ST + 64;
constexpr int L_FL = L_G + 16;
constexpr int L_BYTES = L_FL + 32;

__device__ __forceinline__ rsrc_t mk_rsrc(const void* p, int bytes) { return __builtin_amdgcn_make_buffer_rsrc((void*)p, 0, bytes, 0x00020000); }
__device__ __forceinline__ v4u mk_desc(unsigned long long a, int bytes) { return v4u{(unsigned)a, (unsigned)(a >> 32) & 0xffffu, (unsigned)bytes, 0x00020000u}; }
__device__ __forceinline__ unsigned pk_bf16(float lo, float hi) { v2bf r = {(__bf16)lo, (__bf16)hi}; return __builtin_bit_cast(unsigned, r); }
__device__ __forceinline__ float rbf(float x) { return (float)(__bf16)x; }
__device__ __forceinline__ float bf_lo(unsigned u) { return __builtin_bit_cast(float, u << 16); }
__device__ __forceinline__ float bf_hi(unsigned u) { return __builtin_bit_cast(float, u & 0xffff0000u); }
__device__ __forceinline__ float dot2(unsigned a, unsigned b, float c) { return __builtin_amdgcn_fdot2_f32_bf16(__builtin_bit_cast(v2bf, a), __builtin_bit_cast(v2bf, b), c, false); }
__device__ __forceinline__ v4f mfma(v4u a, v4u b, v4f c) { return __builtin_amdgcn_mfma_f32_16x16x32_bf16(__builtin_bit_cast(v8bf, a), __builtin_bit_cast(v8bf, b), c, 0, 0, 0); }
__device__ __forceinline__ v4f smfma(v4u a, v8u b, v4f c, int idx) { return __builtin_amdgcn_smfmac_f32_16x16x64_bf16(__builtin_bit_cast(v8bf, a), __builtin_bit_cast(v16bf, b), c, idx, 0, 0); }
template <int CTRL> __device__ __forceinline__ unsigned dppu(unsigned x) { return (unsigned)__builtin_amdgcn_mov_dpp((int)x, CTRL, 0xf, 0xf, true); }
template <int CTRL> __device__ __forceinline__ float dppf(float x) { return __builtin_bit_cast(float, dppu<CTRL>(__builtin_bit_cast(unsigned, x))); }
constexpr int XOR1 = 0xB1, XOR2 = 0x4E, XOR7 = 0x141, XOR8 = 0x128, ROR4 = 0x124, BCAST = 0x150;
__device__ __forceinline__ float swap32_sum(float x) {
  auto t = __builtin_amdgcn_permlane32_swap(__float_as_uint(x), __float_as_uint(x), false, false);
  return __uint_as_float(t[0]) + __uint_as_float(t[1]);
}
__device__ __forceinline__ float rows_sum(float x) {
  auto s = __builtin_amdgcn_permlane16_swap(__float_as_uint(x), __float_as_uint(x), false, false);
  return swap32_sum(__uint_as_float(s[0]) + __uint_as_float(s[1]));
}
__device__ __forceinline__ float rows_max(float x) {
  auto s = __builtin_amdgcn_permlane16_swap(__float_as_uint(x), __float_as_uint(x), false, false);
  x = fmaxf(__uint_as_float(s[0]), __uint_as_float(s[1]));
  auto t = __builtin_amdgcn_permlane32_swap(__float_as_uint(x), __float_as_uint(x), false, false);
  return fmaxf(__uint_as_float(t[0]), __uint_as_float(t[1]));
}
__device__ __forceinline__ float wave_sum(float x) { x += dppf<XOR8>(x); x += dppf<XOR7>(x); x += dppf<XOR2>(x); x += dppf<XOR1>(x); return rows_sum(x); }
__device__ __forceinline__ float wave_max(float x) { x = fmaxf(x, dppf<XOR8>(x)); x = fmaxf(x, dppf<XOR7>(x)); x = fmaxf(x, dppf<XOR2>(x)); x = fmaxf(x, dppf<XOR1>(x)); return rows_max(x); }
template <int N> __device__ __forceinline__ void wait_vm() { asm volatile("s_waitcnt vmcnt(%0)" ::"i"(N) : "memory"); }
__device__ __forceinline__ void barrier() { asm volatile("s_waitcnt lgkmcnt(0)\n\ts_barrier" ::: "memory"); }
__device__ __forceinline__ unsigned lds_peek(LAS unsigned* p) { return __builtin_amdgcn_readfirstlane(__hip_atomic_load(p, __ATOMIC_RELAXED, __HIP_MEMORY_SCOPE_WORKGROUP)); }
__device__ __forceinline__ void lds_wait_ge(LAS unsigned* p, unsigned want) {
  while (lds_peek(p) < want) __builtin_amdgcn_s_sleep(2);
  asm volatile("" ::: "memory");
}
__device__ __forceinline__ void lds_signal(LAS unsigned* p, int lane) {
  asm volatile("s_waitcnt lgkmcnt(0)" ::: "memory");
  if (lane == 0) __hip_atomic_fetch_add(p, 1u, __ATOMIC_RELAXED, __HIP_MEMORY_SCOPE_WORKGROUP);
  asm volatile("" ::: "memory");
}
__device__ __forceinline__ v4u tag_bytes(unsigned tag) { return v4u{tag & 0xffu, (tag >> 8) & 0xffu, (tag >> 16) & 0xffu, tag >> 24}; }
__device__ __forceinline__ v4u tag4(float y, v4u tb) {
  const unsigned u = (__float_as_uint(y) + 0x80u) & 0xffffff00u;
  return v4u{u | tb.x, dppu<0x55>(u) | tb.y, dppu<0xAA>(u) | tb.z, dppu<0xFF>(u) | tb.w};
}
__device__ __forceinline__ bool has_tag(v4u g, unsigned tag) {
  return __builtin_amdgcn_perm(__builtin_amdgcn_perm(g.w, g.z, 0x0c0c0400u), __builtin_amdgcn_perm(g.y, g.x, 0x0c0c0400u), 0x05040100u) == tag;
}
__device__ __forceinline__ v4f untag(v4u g) { return __builtin_bit_cast(v4f, g & 0xffffff00u); }
#define STUB(n) ".p2align 12\n.Lgm_page" #n ":\n\ts_setpc_b64 s[60:61]\n"
#define PAGE(n) asm volatile("s_branch .Lgm_after" #n "\n\t" STUB(n) ".Lgm_after" #n ":" ::: "memory")
__device__ __forceinline__ void visit(int which) {
  asm volatile("s_cmp_eq_u32 %0, 13\n\ts_cselect_b32 s60, .Lgm_page1-.Lgm_from, 0\n\ts_cmp_eq_u32 %0, 14\n\ts_cselect_b32 s60, .Lgm_page2-.Lgm_from, s60\n\t"
               "s_cmp_eq_u32 %0, 15\n\ts_cselect_b32 s60, .Lgm_page3-.Lgm_from, s60\n\ts_getpc_b64 s[62:63]\n.Lgm_from:\n\t"
               "s_add_u32 s62, s62, s60\n\ts_addc_u32 s63, s63, 0\n\ts_swappc_b64 s[60:61], s[62:63]" ::"s"(which) : "s60", "s61", "s62", "s63", "scc", "memory");
}
#define SB() __builtin_amdgcn_sched_barrier(0)
#define PIN_S(x) asm volatile("" : "+s"(x))
constexpr int rev4(int i) { return ((i & 1) << 3) | ((i & 2) << 1) | ((i & 4) >> 1) | ((i & 8) >> 3); }

struct Stream {
  rsrc_t rk, rv, rws;
  int voff, so, lane, w, tpo, lag;
  LAS unsigned char* L;
  v4u a[64], q, tp;
  float lv[4], s[2], m, fac;
  unsigned pp[2], msk[4], tag, tp0;
  v4f acc;
};
constexpr int k_done(int n) { int d = n < N0 ? 0 : 2 * (n - N0 + 1); int lim = n - RHI + 1; d = d < lim ? d : lim; return d < 0 ? 0 : (d > 32 ? 32 : d); }
constexpr int soft_step() { int n = 0; while (k_done(n) < 32) ++n; return n + 1; }
constexpr int v_done(int n) { int d = n <= soft_step() ? 0 : 2 * (n - soft_step()); int lim = n - RHI + 1 - 32; d = d < lim ? d : lim; return d < 0 ? 0 : (d > 32 ? 32 : d); }
constexpr int sched(int n) { return ((n < NB ? n : NB) * DT + (n < NB ? 0 : n - NB) * DT2) / 16; }
constexpr int NC = NP + DR + RHI + 1;

template <int I> __device__ __forceinline__ void eat_k(Stream& S) {
  if constexpr (I < R0) S.a[I] = *(LAS v4u*)(S.L + L_K0 + (S.w * R0 + I) * 1024 + S.lane * 16);
  float d = dot2(S.a[I].x, S.q.x, 0.f);
  d = dot2(S.a[I].y, S.q.y, d); d = dot2(S.a[I].z, S.q.z, d); d = dot2(S.a[I].w, S.q.w, d);
  const int c = S.lane;
#define TR_LEVEL(LV, CTRL, BIT) \
  if constexpr ((I & (1 << LV)) == 0) { S.lv[LV] = d; return; } \
  { const float t1 = S.lv[LV] + dppf<CTRL>(S.lv[LV]), t2 = d + dppf<CTRL>(d); d = (c & BIT) ? t2 : t1; }
  TR_LEVEL(0, XOR8, 8) TR_LEVEL(1, XOR7, 4) TR_LEVEL(2, XOR2, 2) TR_LEVEL(3, XOR1, 1)
#undef TR_LEVEL
  S.s[I >> 4] = d;
}
template <int I> __device__ __forceinline__ void eat_v(Stream& S) {
  const unsigned t = dppu<BCAST + rev4(I & 15)>(S.pp[I >> 4]);
  const v4u cf = {t & S.msk[0], t & S.msk[1], t & S.msk[2], t & S.msk[3]};
  S.acc = mfma(cf, S.a[32 + I], S.acc);
}
template <int LO, int HI> __device__ __forceinline__ void eat_ks(Stream& S) { if constexpr (LO < HI) { eat_k<LO>(S); eat_ks<LO + 1, HI>(S); } }
template <int LO, int HI> __device__ __forceinline__ void eat_vs(Stream& S) { if constexpr (LO < HI) { eat_v<LO>(S); SB(); eat_vs<LO + 1, HI>(S); } }

template <int N> __device__ __forceinline__ void stream_step(Stream& S) {
  if constexpr (N < 64) {
    LAS unsigned* fl = (LAS unsigned*)(S.L + L_FL);
    if constexpr (N == 37) PAGE(1);
    if constexpr (N >= NC) {
      if constexpr ((N - NC) % 3 == 0) {
        const unsigned org = lds_peek(fl + 4);
        const int lag = (int)((unsigned)__builtin_amdgcn_s_memrealtime() - org) - (TA + sched(N));
        S.lag = org ? lag : -1;
      }
      asm volatile("s_cmpk_gt_i32 %0, %1\n\ts_cbranch_scc1 1f\n\ts_cmpk_gt_i32 %0, %2\n\ts_cbranch_scc1 2f\n\t"
                   "s_waitcnt vmcnt(%3)\n2:\n\ts_waitcnt vmcnt(%4)\n1:\n\ts_waitcnt vmcnt(%5)"
                   ::"s"(S.lag), "i"(THI), "i"(-TLO & 0xffff), "i"(RLO - 1), "i"(R - 1), "i"(RHI - 1) : "memory", "scc");
    } else if constexpr (N >= R) wait_vm<R - 1>();
    if constexpr (N == 32) S.so = 0;
    S.a[N] = __builtin_amdgcn_raw_buffer_load_b128(N < 32 ? S.rk : S.rv, S.voff, S.so, AUX_NT);
    SB();
    if constexpr (N != 31 && N != 63) asm volatile("s_addk_i32 %0, 0x2000" : "+s"(S.so)::"scc");
    {
      if constexpr (N == NP) {
        if (S.w == 0) {
          S.tp0 = (unsigned)__builtin_amdgcn_s_memrealtime();
          lds_wait_ge(fl + 0, 8u);
          S.tag = *(LAS unsigned*)(S.L + L_EP + (S.lane >> 3) * 16) + TAG_STEP;
          __builtin_amdgcn_raw_buffer_store_b128(tag_bytes(S.tag) | (S.tp0 << 8), S.rws, S.lane == ((S.tpo >> 4) & 63) ? WS_TP + S.tpo : OOB, 0, AUX_SC1);
        }
      }
      if constexpr (N == NP + DR) { if (S.w == 0) S.tp = __builtin_amdgcn_raw_buffer_load_b128(S.rws, WS_TP + (S.tpo & ~1023) + S.lane * 16, 0, AUX_SC1); }
      if constexpr (N == NC - 1) {
        if (S.w == 0) {
          const bool ok = has_tag(S.tp, S.tag);
          const float dsum = wave_sum(ok ? (float)((int)(S.tp.x - (S.tp0 << 8)) >> 8) : 0.f), cnt = (float)__builtin_popcountll(__ballot(ok));
          const unsigned org = (S.tp0 + (int)(dsum * __builtin_amdgcn_rcpf(fmaxf(cnt, 1.f))) - sched(NP)) | 1u;
          if (S.lane == 0) __hip_atomic_store(fl + 4, org, __ATOMIC_RELAXED, __HIP_MEMORY_SCOPE_WORKGROUP);
        }
      }
    }
    if constexpr (N == N0) {
      lds_wait_ge(fl + 1, 1u);
      S.q = *(LAS v4u*)(S.L + L_Q + (S.lane & 15) * 16);
    }
    eat_ks<k_done(N - 1), k_done(N)>(S);
    if constexpr (N == soft_step()) {
      constexpr float C2 = 0.08838834764831845f * 1.4426950408889634f;
      const float s0 = S.s[0] * C2, s1 = S.s[1] * C2;
      S.m = wave_max(fmaxf(s0, s1));
      const float p0 = __builtin_amdgcn_exp2f(s0 - S.m), p1 = __builtin_amdgcn_exp2f(s1 - S.m);
      S.pp[0] = pk_bf16(p0, p0); S.pp[1] = pk_bf16(p1, p1);
      const float l = wave_sum(bf_lo(S.pp[0]) + bf_lo(S.pp[1]));
      if (S.lane == 0) *(LAS v2f*)(S.L + L_ST + S.w * 8) = v2f{S.m, l};
      lds_signal(fl + 3, S.lane);
    }
    if constexpr (N == NF) {
      lds_wait_ge(fl + 3, 8u);
      lds_wait_ge(fl + 2, 1u);
      const v2f st = *(LAS v2f*)(S.L + L_ST + (S.lane & 7) * 8);
      float M = fmaxf(st.x, dppf<XOR7>(st.x)); M = fmaxf(M, dppf<XOR2>(M)); M = fmaxf(M, dppf<XOR1>(M));
      float Ls = __builtin_amdgcn_exp2f(st.x - M) * st.y; Ls += dppf<XOR7>(Ls); Ls += dppf<XOR2>(Ls); Ls += dppf<XOR1>(Ls);
      S.fac = __builtin_amdgcn_exp2f(S.m - M) * *(LAS float*)(S.L + L_G) / Ls;
      asm volatile("" : "+v"(S.fac));
    }
    eat_vs<v_done(N - 1), v_done(N)>(S);
    SB();
    stream_step<N + 1>(S);
  }
}

__device__ __forceinline__ void to_frags(LAS unsigned char* tr, v4u* fr) {
#pragma unroll
  for (int t = 0; t < 4; ++t) {
    const v2u lo = __builtin_bit_cast(v2u, __builtin_amdgcn_ds_read_tr16_b64_v4i16((LAS v4s*)(tr + t * 32)));
    const v2u hi = __builtin_bit_cast(v2u, __builtin_amdgcn_ds_read_tr16_b64_v4i16((LAS v4s*)(tr + t * 32 + 512)));
    fr[t] = v4u{lo.x, lo.y, hi.x, hi.y};
  }
  asm volatile("" : "+v"(fr[0]), "+v"(fr[1]), "+v"(fr[2]), "+v"(fr[3])::"memory");
}
template <int I, int AUX> __device__ __forceinline__ void dma(int m0v, int voff, v4u desc, int soff) {
#define DMA(POL) asm volatile("s_mov_b32 m0, %0\n\ts_nop 0\n\tbuffer_load_dwordx4 %1, %2, %3 offen offset:%4 " POL " lds" ::"s"(m0v), "v"(voff), "s"(desc), "s"(soff - I * 1024), "i"(I * 1024) : "memory")
  if constexpr (AUX == AUX_NT) DMA("nt"); else if constexpr (AUX == AUX_SC1) DMA("sc1"); else DMA("");
#undef DMA
}
template <int N, class FQ, class FH> __device__ __forceinline__ void weight_step(int stg, LAS unsigned char* tr, int voff, v4u d2, v4u dO, v4u* fr, LAS unsigned* late, FQ& gather_q, FH& gather_h) {
  if constexpr (N < 22) {
    if constexpr (N == 13) PAGE(3);
    if constexpr (N == NQ) gather_q();
    if constexpr (N == NG) gather_h();
    if constexpr (N % 4 == 0) {
      wait_vm<0>();
      if constexpr (N > 0 && N <= 16) to_frags(tr + ((N / 4) & 1) * 4096, fr + N - 4);
    } else if constexpr (N % 4 == 2) {
      if (N < 8 || lds_peek(late) == 0u) wait_vm<0>();
    }
    dma<N % 4, AUX_NT>(stg + ((N / 4 + 1) & 1) * 4096, voff, N < 16 ? dO : d2, (N < 16 ? N : N - 16) * 8192);
    SB();
    weight_step<N + 1>(stg, tr, voff, d2, dO, fr, late, gather_q, gather_h);
  }
}

__global__ void __launch_bounds__(1024, 4) __attribute__((aligned(4096)))
fused_kernel(const void* x_, const void* kc_, const void* vc_, const void* wq_, const void* w1_, const void* w3_, void* ws_, const void* wo_,
             const void* w2_, const void* gw_, void* out_) {
  __shared__ __attribute__((aligned(16))) unsigned char smem[L_BYTES];
  LAS unsigned char* L = (LAS unsigned char*)smem;
  v8ul ka;
  v2ul kb;
  unsigned long long outp;
  asm volatile("s_load_dwordx16 %0, %3, 0x0\n\ts_load_dwordx4 %1, %3, 0x40\n\ts_load_dwordx2 %2, %3, 0x50" : "=&s"(ka), "=&s"(kb), "=&s"(outp) : "s"(__builtin_amdgcn_kernarg_segment_ptr()));
  SB();
  const int tid = threadIdx.x, lane = tid & 63, w16 = __builtin_amdgcn_readfirstlane(tid >> 6), w = w16 & 7;
  if (w16 >= 13) visit(w16);
  const int bid = blockIdx.x, xcd = bid & 7, j = bid >> 3, h = 4 * xcd + (j & 3), b = j >> 2;
  const int r = lane >> 4, c = lane & 15;
  const bool streamer = w16 >= 8;
  const bool isq = j < 16, valid = j < 27;
  const int qm = (j - 16) >> 31;
  const int un = (11 + (qm & 5)) * xcd + (j & 15), par = un & 1, mat = (un >> 1) & 1, sl = un >> (2 + qm);
  const int ustr = 2 * FF + (qm & (8192 - 2 * FF));
  const int col0 = 64 * sl - (~qm & 32 * par);
  const int c0 = h * 43 / 8, nch = (h + 1) * 43 / 8 - c0;
  const size_t kvbase = ((size_t)(b * NH + h) * SK + (SK - WIN)) * 256;
  const int cb = col0 * 2 + (lane & 7) * 16;
  int vu = (2 * (32 * w + (lane >> 3)) + par) * ustr + cb;
  asm volatile("" : "+v"(vu));
  vu = (unsigned)cb < (unsigned)ustr ? vu : OOB;
  int vo0 = lane * 16 + w * 1024;
  asm volatile("" : "+v"(vu), "+v"(vo0));
  vo0 = streamer ? vo0 : vu;
  const int nb0 = streamer ? WIN * 256 : (valid ? DM * ustr : 0), st0 = streamer ? 8192 : 16 * ustr;
  const int m00 = streamer ? L_K0 + w * R0 * 1024 : L_STG + w * 8192;
  asm volatile("s_waitcnt lgkmcnt(0)" : "+s"(ka), "+s"(kb), "+s"(outp), "+v"(vo0) : "s"(nb0), "s"(st0), "s"(m00), "s"(kvbase));
  {
    unsigned long long p0 = mat ? ka[5] : ka[4], pk = ka[1] + kvbase;
    asm volatile("" : "+s"(p0), "+s"(pk));
    p0 = isq ? ka[3] : p0;
    PIN_S(p0);
    const v4u d0 = mk_desc(streamer ? pk : p0, nb0);
    dma<0, AUX_NT>(m00, vo0, d0, 0); dma<1, AUX_NT>(m00, vo0, d0, st0);
    SB();
    if (!streamer) {
      dma<2, AUX_NT>(m00, vo0, d0, 2 * st0); dma<3, AUX_NT>(m00, vo0, d0, 3 * st0);
      dma<0, 0>(L_X + w * 1024, (lane >> 3) * (DM * 2) + (lane & 7) * 16, mk_desc(ka[0], 8 * DM * 2), w * 128);
      unsigned long long tp = ka[6], t1 = ka[2] + kvbase, t2 = ka[7] + h * 131072, t3 = kb[0] + c0 * 8192, t4 = outp + (b * DM + 16 * h) * 2;
      int tn = w == 7 ? 1024 : 0;
      asm volatile("" : "+s"(t1), "+s"(t2), "+s"(t3), "+s"(t4), "+s"(tn));
      tp = w == 1 ? t1 : tp; PIN_S(tp);
      tp = w == 2 ? t2 : tp; PIN_S(tp);
      tp = w == 3 ? t3 : tp; PIN_S(tp);
      tp = w == 4 ? t4 : tp;
      const v4u dt = mk_desc(tp, (unsigned)(w - 1) < 4u ? 16 : tn);
      const int vt = lane < 8 ? lane * 128 : OOB;
      if (w == 7) dma<0, AUX_SC1>(L_EP, vt, dt, 0); else dma<0, AUX_NT>(L_RED + w * 2048, vt, dt, 0);
    }
  }
  if (tid < 8) ((LAS unsigned*)(L + L_FL))[tid] = 0u;
  barrier();
  int role = w16 >> 3;
  PIN_S(role);
  if (role == 0) {
    LAS unsigned* fl = (LAS unsigned*)(L + L_FL);
    const rsrc_t rws = mk_rsrc((void*)ka[6], WS_BYTES);
    LAS unsigned char* tr = L + L_STG + w * 8192 + (8 * r + (c >> 2)) * 128 + (c & 3) * 8;
    {
      v4u uf[4];
      wait_vm<0>();
      to_frags(tr, uf);
      const unsigned ps = par ? 0x07060302u : 0x05040100u;
      const v4u x0 = *(LAS v4u*)(L + L_X + w * 1024 + (c & 7) * 128 + r * 32), x1 = *(LAS v4u*)(L + L_X + w * 1024 + (c & 7) * 128 + r * 32 + 16);
      const v4u xa = {__builtin_amdgcn_perm(x0.y, x0.x, ps), __builtin_amdgcn_perm(x0.w, x0.z, ps), __builtin_amdgcn_perm(x1.y, x1.x, ps), __builtin_amdgcn_perm(x1.w, x1.z, ps)};
#pragma unroll
      for (int tt = 0; tt < 4; ++tt) {
        const v4f ua = mfma(xa, uf[tt], v4f{0.f, 0.f, 0.f, 0.f});
        if (lane < 32) *(LAS v4f*)(L + L_RED + w * 2048 + tt * 512 + lane * 16) = ua;
      }
      lds_signal(fl + 0, lane);
      lds_wait_ge(fl + 0, 8u);
      const int ro = L_RED + r * 512 + ((w >> 2) * 16 + c) * 16 + (w & 3) * 4;
      float sum = 0.f;
#pragma unroll
      for (int ww = 0; ww < 8; ++ww) sum += *(LAS float*)(L + ro + ww * 2048);
      const int col = col0 + lane;
      const bool on = valid && !(lane & 3) && (isq || (unsigned)col < (unsigned)FF);
      __builtin_amdgcn_raw_buffer_store_b128(tag4(sum, tag_bytes(*(LAS unsigned*)(L + L_EP + w * 16) + TAG_STEP)), rws,
                                             on ? WS_PX + par * PX_HALF + (w * NCOL + (isq ? 0 : 4096 + mat * FF) + col) * 4 : OOB, 0, AUX_SC1_NT);
    }
    const unsigned tag = *(LAS unsigned*)(L + L_EP + b * 16) + TAG_STEP;
    auto gather_q = [&]() {
      if (w == 0) {
        v4u g;
        int po = WS_PX + (lane >> 5) * PX_HALF + (b * NCOL + h * 128 + 4 * (lane & 31)) * 4;
#pragma unroll 1
        for (unsigned spins = 0; spins < SPIN_CAP; ++spins) {
          asm volatile("" : "+v"(po)::"memory");
          g = __builtin_amdgcn_raw_buffer_load_b128(rws, po, 0, AUX_SC1_NT);
          if (__all(has_tag(g, tag))) break;
        }
        const v4f v = untag(g);
        const v2u qq = {pk_bf16(swap32_sum(v.x), swap32_sum(v.y)), pk_bf16(swap32_sum(v.z), swap32_sum(v.w))};
        if (lane < 32) *(LAS v2u*)(L + L_Q + lane * 8) = qq;
        lds_signal(fl + 1, lane);
      }
    };
    auto gather_h = [&]() {
      if (w == 1) {
        const v4u gx = __builtin_amdgcn_raw_buffer_load_b128(mk_rsrc((void*)ka[0], 8 * DM * 2), b * DM * 2 + lane * 16, 0, 0);
        const v4u gg = __builtin_amdgcn_raw_buffer_load_b128(mk_rsrc((void*)kb[1], DM * 2), lane * 16, 0, 0);
        const bool on = c < 2 * nch;
        int po = on ? WS_PX + (r >> 1) * PX_HALF + (b * NCOL + 4096 + (r & 1) * FF + 8 * c0 + 4 * c) * 4 : OOB;
        v4u gu;
#pragma unroll 1
        for (unsigned spins = 0; spins < SPIN_CAP; ++spins) {
          asm volatile("" : "+v"(po)::"memory");
          gu = __builtin_amdgcn_raw_buffer_load_b128(rws, po, 0, AUX_SC1_NT);
          if (__all(!on || has_tag(gu, tag))) break;
        }
        float s = 0.f;
#pragma unroll
        for (int d = 0; d < 4; ++d) s += rbf(bf_lo(gx[d]) * bf_lo(gg[d])) + rbf(bf_hi(gx[d]) * bf_hi(gg[d]));
        s = rbf(wave_sum(s));
        const float g = rbf(1.0f / (1.0f + __expf(-s)));
        if (lane == 0) *(LAS v2f*)(L + L_G) = v2f{g, rbf(1.0f - g)};
        const v4f pv = untag(gu);
        float hv[4];
#pragma unroll
        for (int e = 0; e < 4; ++e) {
          const float t = rbf(swap32_sum(pv[e]));
          auto sw = __builtin_amdgcn_permlane16_swap(__float_as_uint(t), __float_as_uint(t), false, false);
          const float gv = __uint_as_float(sw[0]), uv = __uint_as_float(sw[1]);
          hv[e] = rbf(gv / (1.0f + __expf(-gv))) * uv;
        }
        if (lane < 16) *(LAS v2u*)(L + L_H + lane * 8) = v2u{pk_bf16(hv[0], hv[1]), pk_bf16(hv[2], hv[3])};
        lds_signal(fl + 2, lane);
        lds_wait_ge(fl + 1, 1u);
        __builtin_amdgcn_raw_buffer_store_b128(tag_bytes(tag), rws, lane == 0 ? WS_AK + (b * 32 + h) * 16 : OOB, 0, AUX_SC1);
      }
    };
    v4u wf[16];
    weight_step<0>(L_STG + w * 8192, tr, (lane >> 3) * 1024 + w * 128 + (lane & 7) * 16, mk_desc(kb[0] + c0 * 8192, nch * 8192), mk_desc(ka[7] + h * 131072, 131072),
                   wf, fl + 3, gather_q, gather_h);
    wait_vm<0>();
    v4f accb[4];
    {
      lds_wait_ge(fl + 2, 1u);
      v4u h0 = *(LAS v4u*)(L + L_H + r * 16), h1 = *(LAS v4u*)(L + L_H + 64 + r * 16);
      if (c) h0 = h1 = v4u{0u, 0u, 0u, 0u};
      const float omg = *(LAS float*)(L + L_G + 4);
      v4u f2[4];
      to_frags(tr + 4096, f2);
#pragma unroll
      for (int tt = 0; tt < 4; ++tt) accb[tt] = mfma(h0, f2[tt], v4f{0.f, 0.f, 0.f, 0.f});
      to_frags(tr, f2);
#pragma unroll
      for (int tt = 0; tt < 4; ++tt) accb[tt] = mfma(h1, f2[tt], accb[tt]) * omg;
    }
    int fo = (lane & 3) ? OOB : WS_FX + ((b * 32 + 4 * w + r) * 32 + h) * 64 + c * 4;
    int io = WS_FX + (b * 32 + h) * 2048 + lane * 16;
    int oo = lane < 4 ? (b * DM + 16 * h + 4 * lane) * 2 : OOB;
    const rsrc_t rout = mk_rsrc((void*)outp, 8 * DM * 2);
    LAS unsigned char* ap = L + L_OP + (c & 7) * OPP + r * 32 + (c >> 3) * 16;
    int sidx = (c >> 3) ? 0xEEEE : 0x4444;
    v8u ww[8];
#pragma unroll
    for (int i = 0; i < 8; ++i) ww[i] = __builtin_shufflevector(wf[(i >> 2) * 8 + (i & 3)], wf[(i >> 2) * 8 + 4 + (i & 3)], 0, 1, 2, 3, 4, 5, 6, 7);
    asm volatile("" : "+v"(ww[0]), "+v"(ww[1]), "+v"(ww[2]), "+v"(ww[3]), "+v"(ww[4]), "+v"(ww[5]), "+v"(ww[6]), "+v"(ww[7]), "+v"(sidx));
    v4u tb = tag_bytes(tag);
    asm volatile("" : "+v"(fo), "+v"(io), "+v"(oo), "+v"(tb), "+v"(ap));
    asm volatile("" : "+v"(accb[0]), "+v"(accb[1]), "+v"(accb[2]), "+v"(accb[3]));
    if (w == 3 && h == 0) {
      int po = lane < 32 ? WS_AK + (b * 32 + lane) * 16 : OOB;
#pragma unroll 1
      for (unsigned spins = 0; spins < SPIN_CAP; ++spins) {
        asm volatile("" : "+v"(po)::"memory");
        const v4u g = __builtin_amdgcn_raw_buffer_load_b128(rws, po, 0, AUX_SC1);
        if (__all(lane >= 32 || has_tag(g, tag))) break;
      }
      __builtin_amdgcn_raw_buffer_store_b32(tag, rws, lane == 0 ? WS_EP + b * 128 : OOB, 0, AUX_SC1);
    }
    barrier();
    {
      const v4u oa0 = *(LAS v4u*)ap, oa1 = *(LAS v4u*)(ap + 128);
      SB();
#pragma unroll
      for (int tt = 0; tt < 4; ++tt) accb[tt] = smfma(oa0, ww[tt], accb[tt], sidx);
#pragma unroll
      for (int tt = 0; tt < 4; ++tt) accb[tt] = smfma(oa1, ww[4 + tt], accb[tt], sidx);
    }
    {
      float sm[4];
#pragma unroll
      for (int tt = 0; tt < 4; ++tt) { sm[tt] = accb[tt].x + accb[tt].y; float t2 = accb[tt].z + accb[tt].w; asm volatile("" : "+v"(sm[tt]), "+v"(t2)); sm[tt] += t2; }
      auto s02 = __builtin_amdgcn_permlane32_swap(__float_as_uint(sm[0]), __float_as_uint(sm[2]), false, false);
      auto s13 = __builtin_amdgcn_permlane32_swap(__float_as_uint(sm[1]), __float_as_uint(sm[3]), false, false);
      auto sw = __builtin_amdgcn_permlane16_swap(__float_as_uint(__uint_as_float(s02[0]) + __uint_as_float(s02[1])), __float_as_uint(__uint_as_float(s13[0]) + __uint_as_float(s13[1])), false, false);
      __builtin_amdgcn_raw_buffer_store_b128(tag4(__uint_as_float(sw[0]) + __uint_as_float(sw[1]), tb), rws, fo, 0, AUX_SC1);
    }
    if (w == 0) {
      v4f y4 = {0.f, 0.f, 0.f, 0.f};
      int o0 = io, o1 = io;
      __builtin_amdgcn_s_sleep(PD);
#pragma unroll 1
      for (unsigned spins = 0; spins < SPIN_CAP; ++spins) {
        asm volatile("" : "+v"(o0), "+v"(o1)::"memory");
        const v4u g0 = __builtin_amdgcn_raw_buffer_load_b128(rws, o0, 0, AUX_SC1_NT), g1 = __builtin_amdgcn_raw_buffer_load_b128(rws, o1 + 1024, 0, AUX_SC1_NT);
        const bool got0 = o0 < OOB && has_tag(g0, tag), got1 = o1 < OOB && has_tag(g1, tag);
        y4 += got0 ? untag(g0) : v4f{0.f, 0.f, 0.f, 0.f};
        y4 += got1 ? untag(g1) : v4f{0.f, 0.f, 0.f, 0.f};
        o0 = got0 ? OOB : o0; o1 = got1 ? OOB : o1;
        if (__all(o0 >= OOB && o1 >= OOB)) break;
      }
#pragma unroll
      for (int e = 0; e < 4; ++e) { float t = y4[e]; t += dppf<XOR8>(t); t += dppf<ROR4>(t); y4[e] = rows_sum(t); }
      __builtin_amdgcn_raw_buffer_store_b64(v2u{pk_bf16(y4.x, y4.y), pk_bf16(y4.z, y4.w)}, rout, oo, 0, 0);
    }
    __builtin_amdgcn_endpgm();
  } else {
    Stream S;
    S.rk = mk_rsrc((void*)(ka[1] + kvbase), WIN * 256); S.rv = mk_rsrc((void*)(ka[2] + kvbase), WIN * 256); S.rws = mk_rsrc((void*)ka[6], WS_BYTES);
    S.voff = lane * 16 + w * 1024; S.so = R0 * 8192; S.lane = lane; S.L = L; S.w = w; S.tpo = ((j & 3) * 64 + b * 8 + xcd) * 16;
    S.acc = v4f{0.f, 0.f, 0.f, 0.f};
#pragma unroll
    for (int d = 0; d < 4; ++d) S.msk[d] = (c >> 1) == d ? ((c & 1) ? 0xffff0000u : 0x0000ffffu) : 0u;
    stream_step<R0>(S);
    LAS unsigned* op = (LAS unsigned*)(L + L_OP + w * OPP) + 8 * (c >> 1) + ((2 * c + r) & 3);
    asm volatile("" : "+v"(op));
    eat_vs<v_done(63), 32>(S);
    if (lane < 32) {
      op[0] = pk_bf16(S.acc.x * S.fac, S.acc.y * S.fac);
      op[4] = pk_bf16(S.acc.z * S.fac, S.acc.w * S.fac);
    }
    barrier();
    asm volatile("s_endpgm\n\t" STUB(2));
    __builtin_unreachable();
  }
}

}

extern "C" void kernel_launch(void* const* d_in, const int* in_sizes, int n_in, void* d_out, int out_size, void* d_ws, size_t ws_size,
                              hipStream_t stream) {
  fused_kernel<<<256, 1024, 0, stream>>>(d_in[0], d_in[1], d_in[2], d_in[3], d_in[6], d_in[7], d_ws, d_in[4], d_in[8], d_in[9], d_out);
}
```

```cpp
#include <hip/hip_runtime.h>
#include <hip/hip_bf16.h>

namespace {

typedef unsigned v4u __attribute__((ext_vector_type(4)));
typedef unsigned v2u __attribute__((ext_vector_type(2)));
typedef unsigned v8u __attribute__((ext_vector_type(8)));
typedef __bf16 v16bf __attribute__((ext_vector_type(16)));
typedef float v4f __attribute__((ext_vector_type(4)));
typedef float v2f __attribute__((ext_vector_type(2)));
typedef short v4s __attribute__((ext_vector_type(4)));
typedef __bf16 v8bf __attribute__((ext_vector_type(8)));
typedef __bf16 v2bf __attribute__((ext_vector_type(2)));
typedef unsigned long long v8ul __attribute__((ext_vector_type(8)));
typedef unsigned long long v2ul __attribute__((ext_vector_type(2)));
#define LAS __attribute__((address_space(3)))
typedef __amdgpu_buffer_rsrc_t rsrc_t;

constexpr int DM = 512, NH = 32, SK = 4096, WIN = 1024, FF = 1376;
constexpr int NCOL = 4096 + 2 * FF;
constexpr int WS_EP = 0, WS_PX = 4096, PX_HALF = 8 * NCOL * 4, WS_TP = 480 * 1024, WS_AK = 488 * 1024, WS_FX = 512 * 1024, WS_BYTES = WS_FX + 8 * 32 * 32 * 16 * 4;
constexpr int AUX_NT = 2, AUX_SC1 = 16, OOB = 0x40000000;
constexpr unsigned TAG_STEP = 0x01010101u;
constexpr unsigned SPIN_CAP = 1u << 16;
constexpr int R0 = 2;
constexpr int R = 3;
constexpr int RHI = 5, THI = 25;
constexpr int RLO = 2, TLO = 30;
constexpr int NP = 20, DR = 7;
constexpr int TA = 20, DT = 500, DT2 = 476, NB = 44;
constexpr int N0 = 20;
constexpr int NF = 52;
constexpr int NQ = 2, NG = 8;
constexpr int PD = 16;
constexpr int L_STG = 0;
constexpr int L_K0 = 65536;
constexpr int L_RED = L_K0 + 8 * R0 * 1024;
constexpr int L_X = L_RED + 16384;
constexpr int OPP = 320;
constexpr int L_OP = L_X + 8192;
constexpr int L_EP = L_OP + 8 * OPP;
constexpr int L_Q = L_EP + 1024;
constexpr int L_H = L_Q + 256;
constexpr int L_ST = L_H + 128;
constexpr int L_G = L_ST + 64;
constexpr int L_FL = L_G + 16;
constexpr int L_BYTES = L_FL + 32;

__device__ __forceinline__ rsrc_t mk_rsrc(const void* p, int bytes) { return __builtin_amdgcn_make_buffer_rsrc((void*)p, 0, bytes, 0x00020000); }
__device__ __forceinline__ v4u mk_desc(unsigned long long a, int bytes) { return v4u{(unsigned)a, (unsigned)(a >> 32) & 0xffffu, (unsigned)bytes, 0x00020000u}; }
__device__ __forceinline__ unsigned pk_bf16(float lo, float hi) { v2bf r = {(__bf16)lo, (__bf16)hi}; return __builtin_bit_cast(unsigned, r); }
__device__ __forceinline__ float rbf(float x) { return (float)(__bf16)x; }
__device__ __forceinline__ float bf_lo(unsigned u) { return __builtin_bit_cast(float, u << 16); }
__device__ __forceinline__ float bf_hi(unsigned u) { return __builtin_bit_cast(float, u & 0xffff0000u); }
__device__ __forceinline__ float dot2(unsigned a, unsigned b, float c) { return __builtin_amdgcn_fdot2_f32_bf16(__builtin_bit_cast(v2bf, a), __builtin_bit_cast(v2bf, b), c, false); }
__device__ __forceinline__ v4f mfma(v4u a, v4u b, v4f c) { return __builtin_amdgcn_mfma_f32_16x16x32_bf16(__builtin_bit_cast(v8bf, a), __builtin_bit_cast(v8bf, b), c, 0, 0, 0); }
__device__ __forceinline__ v4f smfma(v4u a, v8u b, v4f c, int idx) { return __builtin_amdgcn_smfmac_f32_16x16x64_bf16(__builtin_bit_cast(v8bf, a), __builtin_bit_cast(v16bf, b), c, idx, 0, 0); }
template <int CTRL> __device__ __forceinline__ unsigned dppu(unsigned x) { return (unsigned)__builtin_amdgcn_mov_dpp((int)x, CTRL, 0xf, 0xf, true); }
template <int CTRL> __device__ __forceinline__ float dppf(float x) { return __builtin_bit_cast(float, dppu<CTRL>(__builtin_bit_cast(unsigned, x))); }
constexpr int XOR1 = 0xB1, XOR2 = 0x4E, XOR7 = 0x141, XOR8 = 0x128, ROR4 = 0x124, BCAST = 0x150;
__device__ __forceinline__ float swap32_sum(float x) {
  auto t = __builtin_amdgcn_permlane32_swap(__float_as_uint(x), __float_as_uint(x), false, false);
  return __uint_as_float(t[0]) + __uint_as_float(t[1]);
}
__device__ __forceinline__ float rows_sum(float x) {
  auto s = __builtin_amdgcn_permlane16_swap(__float_as_uint(x), __float_as_uint(x), false, false);
  return swap32_sum(__uint_as_float(s[0]) + __uint_as_float(s[1]));
}
__device__ __forceinline__ float rows_max(float x) {
  auto s = __builtin_amdgcn_permlane16_swap(__float_as_uint(x), __float_as_uint(x), false, false);
  x = fmaxf(__uint_as_float(s[0]), __uint_as_float(s[1]));
  auto t = __builtin_amdgcn_permlane32_swap(__float_as_uint(x), __float_as_uint(x), false, false);
  return fmaxf(__uint_as_float(t[0]), __uint_as_float(t[1]));
}
__device__ __forceinline__ float wave_sum(float x) { x += dppf<XOR8>(x); x += dppf<XOR7>(x); x += dppf<XOR2>(x); x += dppf<XOR1>(x); return rows_sum(x); }
__device__ __forceinline__ float wave_max(float x) { x = fmaxf(x, dppf<XOR8>(x)); x = fmaxf(x, dppf<XOR7>(x)); x = fmaxf(x, dppf<XOR2>(x)); x = fmaxf(x, dppf<XOR1>(x)); return rows_max(x); }
template <int N> __device__ __forceinline__ void wait_vm() { asm volatile("s_waitcnt vmcnt(%0)" ::"i"(N) : "memory"); }
__device__ __forceinline__ void barrier() { asm volatile("s_waitcnt lgkmcnt(0)\n\ts_barrier" ::: "memory"); }
__device__ __forceinline__ unsigned lds_peek(LAS unsigned* p) { return __builtin_amdgcn_readfirstlane(__hip_atomic_load(p, __ATOMIC_RELAXED, __HIP_MEMORY_SCOPE_WORKGROUP)); }
__device__ __forceinline__ void lds_wait_ge(LAS unsigned* p, unsigned want) {
  while (lds_peek(p) < want) __builtin_amdgcn_s_sleep(2);
  asm volatile("" ::: "memory");
}
__device__ __forceinline__ void lds_signal(LAS unsigned* p, int lane) {
  asm volatile("s_waitcnt lgkmcnt(0)" ::: "memory");
  if (lane == 0) __hip_atomic_fetch_add(p, 1u, __ATOMIC_RELAXED, __HIP_MEMORY_SCOPE_WORKGROUP);
  asm volatile("" ::: "memory");
}
__device__ __forceinline__ v4u tag_bytes(unsigned tag) { return v4u{tag & 0xffu, (tag >> 8) & 0xffu, (tag >> 16) & 0xffu, tag >> 24}; }
__device__ __forceinline__ v4u tag4(float y, v4u tb) {
  const unsigned u = (__float_as_uint(y) + 0x80u) & 0xffffff00u;
  return v4u{u | tb.x, dppu<0x55>(u) | tb.y, dppu<0xAA>(u) | tb.z, dppu<0xFF>(u) | tb.w};
}
__device__ __forceinline__ bool has_tag(v4u g, unsigned tag) {
  return __builtin_amdgcn_perm(__builtin_amdgcn_perm(g.w, g.z, 0x0c0c0400u), __builtin_amdgcn_perm(g.y, g.x, 0x0c0c0400u), 0x05040100u) == tag;
}
__device__ __forceinline__ v4f untag(v4u g) { return __builtin_bit_cast(v4f, g & 0xffffff00u); }
#define STUB(n) ".p2align 12\n.Lgm_page" #n ":\n\ts_setpc_b64 s[60:61]\n"
#define PAGE(n) asm volatile("s_branch .Lgm_after" #n "\n\t" STUB(n) ".Lgm_after" #n ":" ::: "memory")
__device__ __forceinline__ void visit(int which) {
  asm volatile("s_cmp_eq_u32 %0, 13\n\ts_cselect_b32 s60, .Lgm_page1-.Lgm_from, 0\n\ts_cmp_eq_u32 %0, 14\n\ts_cselect_b32 s60, .Lgm_page2-.Lgm_from, s60\n\t"
               "s_cmp_eq_u32 %0, 15\n\ts_cselect_b32 s60, .Lgm_page3-.Lgm_from, s60\n\ts_getpc_b64 s[62:63]\n.Lgm_from:\n\t"
               "s_add_u32 s62, s62, s60\n\ts_addc_u32 s63, s63, 0\n\ts_swappc_b64 s[60:61], s[62:63]" ::"s"(which) : "s60", "s61", "s62", "s63", "scc", "memory");
}
#define SB() __builtin_amdgcn_sched_barrier(0)
#define PIN_S(x) asm volatile("" : "+s"(x))
constexpr int rev4(int i) { return ((i & 1) << 3) | ((i & 2) << 1) | ((i & 4) >> 1) | ((i & 8) >> 3); }

struct Stream {
  rsrc_t rk, rv, rws;
  int voff, so, lane, w, tpo, lag;
  LAS unsigned char* L;
  v4u a[64], q, tp;
  float lv[4], s[2], m, fac;
  unsigned pp[2], msk[4], tag, tp0;
  v4f acc;
};
constexpr int k_done(int n) { int d = n < N0 ? 0 : 2 * (n - N0 + 1); int lim = n - RHI + 1; d = d < lim ? d : lim; return d < 0 ? 0 : (d > 32 ? 32 : d); }
constexpr int soft_step() { int n = 0; while (k_done(n) < 32) ++n; return n + 1; }
constexpr int v_done(int n) { int d = n <= soft_step() ? 0 : 2 * (n - soft_step()); int lim = n - RHI + 1 - 32; d = d < lim ? d : lim; return d < 0 ? 0 : (d > 32 ? 32 : d); }
constexpr int sched(int n) { return ((n < NB ? n : NB) * DT + (n < NB ? 0 : n - NB) * DT2) / 16; }
constexpr int NC = NP + DR + RHI + 1;

template <int I> __device__ __forceinline__ void eat_k(Stream& S) {
  if constexpr (I < R0) S.a[I] = *(LAS v4u*)(S.L + L_K0 + (S.w * R0 + I) * 1024 + S.lane * 16);
  float d = dot2(S.a[I].x, S.q.x, 0.f);
  d = dot2(S.a[I].y, S.q.y, d); d = dot2(S.a[I].z, S.q.z, d); d = dot2(S.a[I].w, S.q.w, d);
  const int c = S.lane;
#define TR_LEVEL(LV, CTRL, BIT) \
  if constexpr ((I & (1 << LV)) == 0) { S.lv[LV] = d; return; } \
  { const float t1 = S.lv[LV] + dppf<CTRL>(S.lv[LV]), t2 = d + dppf<CTRL>(d); d = (c & BIT) ? t2 : t1; }
  TR_LEVEL(0, XOR8, 8) TR_LEVEL(1, XOR7, 4) TR_LEVEL(2, XOR2, 2) TR_LEVEL(3, XOR1, 1)
#undef TR_LEVEL
  S.s[I >> 4] = d;
}
template <int I> __device__ __forceinline__ void eat_v(Stream& S) {
  const unsigned t = dppu<BCAST + rev4(I & 15)>(S.pp[I >> 4]);
  const v4u cf = {t & S.msk[0], t & S.msk[1], t & S.msk[2], t & S.msk[3]};
  S.acc = mfma(cf, S.a[32 + I], S.acc);
}
template <int LO, int HI> __device__ __forceinline__ void eat_ks(Stream& S) { if constexpr (LO < HI) { eat_k<LO>(S); eat_ks<LO + 1, HI>(S); } }
template <int LO, int HI> __device__ __forceinline__ void eat_vs(Stream& S) { if constexpr (LO < HI) { eat_v<LO>(S); SB(); eat_vs<LO + 1, HI>(S); } }

template <int N> __device__ __forceinline__ void stream_step(Stream& S) {
  if constexpr (N < 64) {
    LAS unsigned* fl = (LAS unsigned*)(S.L + L_FL);
    if constexpr (N == 37) PAGE(1);
    if constexpr (N >= NC) {
      if constexpr ((N - NC) % 3 == 0) {
        const unsigned org = lds_peek(fl + 4);
        const int lag = (int)((unsigned)__builtin_amdgcn_s_memrealtime() - org) - (TA + sched(N));
        S.lag = org ? lag : -1;
      }
      asm volatile("s_cmpk_gt_i32 %0, %1\n\ts_cbranch_scc1 1f\n\ts_cmpk_gt_i32 %0, %2\n\ts_cbranch_scc1 2f\n\t"
                   "s_waitcnt vmcnt(%3)\n2:\n\ts_waitcnt vmcnt(%4)\n1:\n\ts_waitcnt vmcnt(%5)"
                   ::"s"(S.lag), "i"(THI), "i"(-TLO & 0xffff), "i"(RLO - 1), "i"(R - 1), "i"(RHI - 1) : "memory", "scc");
    } else if constexpr (N >= R) wait_vm<R - 1>();
    if constexpr (N == 32) S.so = 0;
    S.a[N] = __builtin_amdgcn_raw_buffer_load_b128(N < 32 ? S.rk : S.rv, S.voff, S.so, AUX_NT);
    SB();
    if constexpr (N != 31 && N != 63) asm volatile("s_addk_i32 %0, 0x2000" : "+s"(S.so)::"scc");
    {
      if constexpr (N == NP) {
        if (S.w == 0) {
          S.tp0 = (unsigned)__builtin_amdgcn_s_memrealtime();
          lds_wait_ge(fl + 0, 8u);
          S.tag = *(LAS unsigned*)(S.L + L_EP + (S.lane >> 3) * 16) + TAG_STEP;
          __builtin_amdgcn_raw_buffer_store_b128(tag_bytes(S.tag) | (S.tp0 << 8), S.rws, S.lane == ((S.tpo >> 4) & 63) ? WS_TP + S.tpo : OOB, 0, AUX_SC1);
        }
      }
      if constexpr (N == NP + DR) { if (S.w == 0) S.tp = __builtin_amdgcn_raw_buffer_load_b128(S.rws, WS_TP + (S.tpo & ~1023) + S.lane * 16, 0, AUX_SC1); }
      if constexpr (N == NC - 1) {
        if (S.w == 0) {
          const bool ok = has_tag(S.tp, S.tag);
          const float dsum = wave_sum(ok ? (float)((int)(S.tp.x - (S.tp0 << 8)) >> 8) : 0.f), cnt = (float)__builtin_popcountll(__ballot(ok));
          const unsigned org = (S.tp0 + (int)(dsum * __builtin_amdgcn_rcpf(fmaxf(cnt, 1.f))) - sched(NP)) | 1u;
          if (S.lane == 0) __hip_atomic_store(fl + 4, org, __ATOMIC_RELAXED, __HIP_MEMORY_SCOPE_WORKGROUP);
        }
      }
    }
    if constexpr (N == N0) {
      lds_wait_ge(fl + 1, 1u);
      S.q = *(LAS v4u*)(S.L + L_Q + (S.lane & 15) * 16);
    }
    eat_ks<k_done(N - 1), k_done(N)>(S);
    if constexpr (N == soft_step()) {
      constexpr float C2 = 0.08838834764831845f * 1.4426950408889634f;
      const float s0 = S.s[0] * C2, s1 = S.s[1] * C2;
      S.m = wave_max(fmaxf(s0, s1));
      const float p0 = __builtin_amdgcn_exp2f(s0 - S.m), p1 = __builtin_amdgcn_exp2f(s1 - S.m);
      S.pp[0] = pk_bf16(p0, p0); S.pp[1] = pk_bf16(p1, p1);
      const float l = wave_sum(bf_lo(S.pp[0]) + bf_lo(S.pp[1]));
      if (S.lane == 0) *(LAS v2f*)(S.L + L_ST + S.w * 8) = v2f{S.m, l};
      lds_signal(fl + 3, S.lane);
    }
    if constexpr (N == NF) {
      lds_wait_ge(fl + 3, 8u);
      lds_wait_ge(fl + 2, 1u);
      const v2f st = *(LAS v2f*)(S.L + L_ST + (S.lane & 7) * 8);
      float M = fmaxf(st.x, dppf<XOR7>(st.x)); M = fmaxf(M, dppf<XOR2>(M)); M = fmaxf(M, dppf<XOR1>(M));
      float Ls = __builtin_amdgcn_exp2f(st.x - M) * st.y; Ls += dppf<XOR7>(Ls); Ls += dppf<XOR2>(Ls); Ls += dppf<XOR1>(Ls);
      S.fac = __builtin_amdgcn_exp2f(S.m - M) * *(LAS float*)(S.L + L_G) / Ls;
      asm volatile("" : "+v"(S.fac));
    }
    eat_vs<v_done(N - 1), v_done(N)>(S);
    SB();
    stream_step<N + 1>(S);
  }
}

__device__ __forceinline__ void to_frags(LAS unsigned char* tr, v4u* fr) {
#pragma unroll
  for (int t = 0; t < 4; ++t) {
    const v2u lo = __builtin_bit_cast(v2u, __builtin_amdgcn_ds_read_tr16_b64_v4i16((LAS v4s*)(tr + t * 32)));
    const v2u hi = __builtin_bit_cast(v2u, __builtin_amdgcn_ds_read_tr16_b64_v4i16((LAS v4s*)(tr + t * 32 + 512)));
    fr[t] = v4u{lo.x, lo.y, hi.x, hi.y};
  }
  asm volatile("" : "+v"(fr[0]), "+v"(fr[1]), "+v"(fr[2]), "+v"(fr[3])::"memory");
}
template <int I, int AUX> __device__ __forceinline__ void dma(int m0v, int voff, v4u desc, int soff) {
#define DMA(POL) asm volatile("s_mov_b32 m0, %0\n\ts_nop 0\n\tbuffer_load_dwordx4 %1, %2, %3 offen offset:%4 " POL " lds" ::"s"(m0v), "v"(voff), "s"(desc), "s"(soff - I * 1024), "i"(I * 1024) : "memory")
  if constexpr (AUX == AUX_NT) DMA("nt"); else if constexpr (AUX == AUX_SC1) DMA("sc1"); else DMA("");
#undef DMA
}
template <int N, class FQ, class FH> __device__ __forceinline__ void weight_step(int stg, LAS unsigned char* tr, int voff, v4u d2, v4u dO, v4u* fr, LAS unsigned* late, FQ& gather_q, FH& gather_h) {
  if constexpr (N < 22) {
    if constexpr (N == 13) PAGE(3);
    if constexpr (N == NQ) gather_q();
    if constexpr (N == NG) gather_h();
    if constexpr (N % 4 == 0) {
      wait_vm<0>();
      if constexpr (N > 0 && N <= 16) to_frags(tr + ((N / 4) & 1) * 4096, fr + N - 4);
    } else if constexpr (N % 4 == 2) {
      if (N < 8 || lds_peek(late) == 0u) wait_vm<0>();
    }
    dma<N % 4, 0>(stg + ((N / 4 + 1) & 1) * 4096, voff, N < 16 ? dO : d2, (N < 16 ? N : N - 16) * 8192);
    SB();
    weight_step<N + 1>(stg, tr, voff, d2, dO, fr, late, gather_q, gather_h);
  }
}

__global__ void __launch_bounds__(1024, 4) __attribute__((aligned(4096)))
fused_kernel(const void* x_, const void* kc_, const void* vc_, const void* wq_, const void* w1_, const void* w3_, void* ws_, const void* wo_,
             const void* w2_, const void* gw_, void* out_) {
  __shared__ __attribute__((aligned(16))) unsigned char smem[L_BYTES];
  LAS unsigned char* L = (LAS unsigned char*)smem;
  v8ul ka;
  v2ul kb;
  unsigned long long outp;
  asm volatile("s_load_dwordx16 %0, %3, 0x0\n\ts_load_dwordx4 %1, %3, 0x40\n\ts_load_dwordx2 %2, %3, 0x50" : "=&s"(ka), "=&s"(kb), "=&s"(outp) : "s"(__builtin_amdgcn_kernarg_segment_ptr()));
  SB();
  const int tid = threadIdx.x, lane = tid & 63, w16 = __builtin_amdgcn_readfirstlane(tid >> 6), w = w16 & 7;
  if (w16 >= 13) visit(w16);
  const int bid = blockIdx.x, xcd = bid & 7, j = bid >> 3, h = 4 * xcd + (j & 3), b = j >> 2;
  const int r = lane >> 4, c = lane & 15;
  const bool streamer = w16 >= 8;
  const bool isq = j < 16, valid = j < 27;
  const int qm = (j - 16) >> 31;
  const int un = (11 + (qm & 5)) * xcd + (j & 15), par = un & 1, mat = (un >> 1) & 1, sl = un >> (2 + qm);
  const int ustr = 2 * FF + (qm & (8192 - 2 * FF));
  const int col0 = 64 * sl - (~qm & 32 * par);
  const int c0 = h * 43 / 8, nch = (h + 1) * 43 / 8 - c0;
  const size_t kvbase = ((size_t)(b * NH + h) * SK + (SK - WIN)) * 256;
  const int cb = col0 * 2 + (lane & 7) * 16;
  int vu = (2 * (32 * w + (lane >> 3)) + par) * ustr + cb;
  asm volatile("" : "+v"(vu));
  vu = (unsigned)cb < (unsigned)ustr ? vu : OOB;
  int vo0 = lane * 16 + w * 1024;
  asm volatile("" : "+v"(vu), "+v"(vo0));
  vo0 = streamer ? vo0 : vu;
  const int nb0 = streamer ? WIN * 256 : (valid ? DM * ustr : 0), st0 = streamer ? 8192 : 16 * ustr;
  const int m00 = streamer ? L_K0 + w * R0 * 1024 : L_STG + w * 8192;
  asm volatile("" : "+v"(vo0));
  asm volatile("s_waitcnt lgkmcnt(0)" : "+s"(ka), "+s"(kb), "+s"(outp) : "s"(nb0), "s"(st0), "s"(m00), "s"(kvbase));
  {
    unsigned long long p0 = mat ? ka[5] : ka[4], pk = ka[1] + kvbase;
    asm volatile("" : "+s"(p0), "+s"(pk));
    p0 = isq ? ka[3] : p0;
    PIN_S(p0);
    const v4u d0 = mk_desc(streamer ? pk : p0, nb0);
    dma<0, AUX_NT>(m00, vo0, d0, 0); dma<1, AUX_NT>(m00, vo0, d0, st0);
    SB();
    if (!streamer) {
      dma<2, AUX_NT>(m00, vo0, d0, 2 * st0); dma<3, AUX_NT>(m00, vo0, d0, 3 * st0);
      dma<0, 0>(L_X + w * 1024, (lane >> 3) * (DM * 2) + (lane & 7) * 16, mk_desc(ka[0], 8 * DM * 2), w * 128);
      if (w == 7) dma<0, AUX_SC1>(L_EP, lane < 8 ? lane * 128 : OOB, mk_desc(ka[6] + WS_EP, 1024), 0);
    }
  }
  if (tid < 8) ((LAS unsigned*)(L + L_FL))[tid] = 0u;
  barrier();
  int role = w16 >> 3;
  PIN_S(role);
  if (role == 0) {
    LAS unsigned* fl = (LAS unsigned*)(L + L_FL);
    const rsrc_t rws = mk_rsrc((void*)ka[6], WS_BYTES);
    LAS unsigned char* tr = L + L_STG + w * 8192 + (8 * r + (c >> 2)) * 128 + (c & 3) * 8;
    {
      v4u uf[4];
      wait_vm<0>();
      to_frags(tr, uf);
      const unsigned ps = par ? 0x07060302u : 0x05040100u;
      const v4u x0 = *(LAS v4u*)(L + L_X + w * 1024 + (c & 7) * 128 + r * 32), x1 = *(LAS v4u*)(L + L_X + w * 1024 + (c & 7) * 128 + r * 32 + 16);
      const v4u xa = {__builtin_amdgcn_perm(x0.y, x0.x, ps), __builtin_amdgcn_perm(x0.w, x0.z, ps), __builtin_amdgcn_perm(x1.y, x1.x, ps), __builtin_amdgcn_perm(x1.w, x1.z, ps)};
#pragma unroll
      for (int tt = 0; tt < 4; ++tt) {
        const v4f ua = mfma(xa, uf[tt], v4f{0.f, 0.f, 0.f, 0.f});
        if (lane < 32) *(LAS v4f*)(L + L_RED + w * 2048 + tt * 512 + lane * 16) = ua;
      }
      lds_signal(fl + 0, lane);
      lds_wait_ge(fl + 0, 8u);
      const int ro = L_RED + r * 512 + ((w >> 2) * 16 + c) * 16 + (w & 3) * 4;
      float sum = 0.f;
#pragma unroll
      for (int ww = 0; ww < 8; ++ww) sum += *(LAS float*)(L + ro + ww * 2048);
      const int col = col0 + lane;
      const bool on = valid && !(lane & 3) && (isq || (unsigned)col < (unsigned)FF);
      __builtin_amdgcn_raw_buffer_store_b128(tag4(sum, tag_bytes(*(LAS unsigned*)(L + L_EP + w * 16) + TAG_STEP)), rws,
                                             on ? WS_PX + par * PX_HALF + (w * NCOL + (isq ? 0 : 4096 + mat * FF) + col) * 4 : OOB, 0, AUX_SC1);
    }
    const unsigned tag = *(LAS unsigned*)(L + L_EP + b * 16) + TAG_STEP;
    auto gather_q = [&]() {
      if (w == 0) {
        v4u g;
        int po = WS_PX + (lane >> 5) * PX_HALF + (b * NCOL + h * 128 + 4 * (lane & 31)) * 4;
#pragma unroll 1
        for (unsigned spins = 0; spins < SPIN_CAP; ++spins) {
          asm volatile("" : "+v"(po)::"memory");
          g = __builtin_amdgcn_raw_buffer_load_b128(rws, po, 0, AUX_SC1);
          if (__all(has_tag(g, tag))) break;
        }
        const v4f v = untag(g);
        const v2u qq = {pk_bf16(swap32_sum(v.x), swap32_sum(v.y)), pk_bf16(swap32_sum(v.z), swap32_sum(v.w))};
        if (lane < 32) *(LAS v2u*)(L + L_Q + lane * 8) = qq;
        lds_signal(fl + 1, lane);
      }
    };
    auto gather_h = [&]() {
      if (w == 1) {
        const v4u gx = __builtin_amdgcn_raw_buffer_load_b128(mk_rsrc((void*)ka[0], 8 * DM * 2), b * DM * 2 + lane * 16, 0, 0);
        const v4u gg = __builtin_amdgcn_raw_buffer_load_b128(mk_rsrc((void*)kb[1], DM * 2), lane * 16, 0, 0);
        const bool on = c < 2 * nch;
        int po = on ? WS_PX + (r >> 1) * PX_HALF + (b * NCOL + 4096 + (r & 1) * FF + 8 * c0 + 4 * c) * 4 : OOB;
        v4u gu;
#pragma unroll 1
        for (unsigned spins = 0; spins < SPIN_CAP; ++spins) {
          asm volatile("" : "+v"(po)::"memory");
          gu = __builtin_amdgcn_raw_buffer_load_b128(rws, po, 0, AUX_SC1);
          if (__all(!on || has_tag(gu, tag))) break;
        }
        float s = 0.f;
#pragma unroll
        for (int d = 0; d < 4; ++d) s += rbf(bf_lo(gx[d]) * bf_lo(gg[d])) + rbf(bf_hi(gx[d]) * bf_hi(gg[d]));
        s = rbf(wave_sum(s));
        const float g = rbf(1.0f / (1.0f + __expf(-s)));
        if (lane == 0) *(LAS v2f*)(L + L_G) = v2f{g, rbf(1.0f - g)};
        const v4f pv = untag(gu);
        float hv[4];
#pragma unroll
        for (int e = 0; e < 4; ++e) {
          const float t = rbf(swap32_sum(pv[e]));
          auto sw = __builtin_amdgcn_permlane16_swap(__float_as_uint(t), __float_as_uint(t), false, false);
          const float gv = __uint_as_float(sw[0]), uv = __uint_as_float(sw[1]);
          hv[e] = rbf(gv / (1.0f + __expf(-gv))) * uv;
        }
        if (lane < 16) *(LAS v2u*)(L + L_H + lane * 8) = v2u{pk_bf16(hv[0], hv[1]), pk_bf16(hv[2], hv[3])};
        lds_signal(fl + 2, lane);
        lds_wait_ge(fl + 1, 1u);
        __builtin_amdgcn_raw_buffer_store_b128(tag_bytes(tag), rws, lane == 0 ? WS_AK + (b * 32 + h) * 16 : OOB, 0, AUX_SC1);
      }
    };
    v4u wf[16];
    weight_step<0>(L_STG + w * 8192, tr, (lane >> 3) * 1024 + w * 128 + (lane & 7) * 16, mk_desc(kb[0] + c0 * 8192, nch * 8192), mk_desc(ka[7] + h * 131072, 131072),
                   wf, fl + 3, gather_q, gather_h);
    wait_vm<0>();
    v4f accb[4];
    {
      lds_wait_ge(fl + 2, 1u);
      v4u h0 = *(LAS v4u*)(L + L_H + r * 16), h1 = *(LAS v4u*)(L + L_H + 64 + r * 16);
      if (c) h0 = h1 = v4u{0u, 0u, 0u, 0u};
      const float omg = *(LAS float*)(L + L_G + 4);
      v4u f2[4];
      to_frags(tr + 4096, f2);
#pragma unroll
      for (int tt = 0; tt < 4; ++tt) accb[tt] = mfma(h0, f2[tt], v4f{0.f, 0.f, 0.f, 0.f});
      to_frags(tr, f2);
#pragma unroll
      for (int tt = 0; tt < 4; ++tt) accb[tt] = mfma(h1, f2[tt], accb[tt]) * omg;
    }
    int fo = (lane & 3) ? OOB : WS_FX + ((b * 32 + 4 * w + r) * 32 + h) * 64 + c * 4;
    int io = WS_FX + (b * 32 + h) * 2048 + lane * 16;
    int oo = lane < 4 ? (b * DM + 16 * h + 4 * lane) * 2 : OOB;
    const rsrc_t rout = mk_rsrc((void*)outp, 8 * DM * 2);
    LAS unsigned char* ap = L + L_OP + (c & 7) * OPP + r * 32 + (c >> 3) * 16;
    int sidx = (c >> 3) ? 0xEEEE : 0x4444;
    v8u ww[8];
#pragma unroll
    for (int i = 0; i < 8; ++i) ww[i] = __builtin_shufflevector(wf[(i >> 2) * 8 + (i & 3)], wf[(i >> 2) * 8 + 4 + (i & 3)], 0, 1, 2, 3, 4, 5, 6, 7);
    asm volatile("" : "+v"(ww[0]), "+v"(ww[1]), "+v"(ww[2]), "+v"(ww[3]), "+v"(ww[4]), "+v"(ww[5]), "+v"(ww[6]), "+v"(ww[7]), "+v"(sidx));
    v4u tb = tag_bytes(tag);
    asm volatile("" : "+v"(fo), "+v"(io), "+v"(oo), "+v"(tb), "+v"(ap));
    asm volatile("" : "+v"(accb[0]), "+v"(accb[1]), "+v"(accb[2]), "+v"(accb[3]));
    if (w == 0) {
      const v4u t0 = __builtin_amdgcn_raw_buffer_load_b128(rws, io, 0, AUX_SC1), t1 = __builtin_amdgcn_raw_buffer_load_b128(rws, io + 1024, 0, AUX_SC1);
      asm volatile("" ::"v"(t0), "v"(t1));
    }
    if (w == 3 && h == 0) {
      int po = lane < 32 ? WS_AK + (b * 32 + lane) * 16 : OOB;
#pragma unroll 1
      for (unsigned spins = 0; spins < SPIN_CAP; ++spins) {
        asm volatile("" : "+v"(po)::"memory");
        const v4u g = __builtin_amdgcn_raw_buffer_load_b128(rws, po, 0, AUX_SC1);
        if (__all(lane >= 32 || has_tag(g, tag))) break;
      }
      __builtin_amdgcn_raw_buffer_store_b32(tag, rws, lane == 0 ? WS_EP + b * 128 : OOB, 0, AUX_SC1);
    }
    barrier();
    {
      const v4u oa0 = *(LAS v4u*)ap, oa1 = *(LAS v4u*)(ap + 128);
      SB();
#pragma unroll
      for (int tt = 0; tt < 4; ++tt) accb[tt] = smfma(oa0, ww[tt], accb[tt], sidx);
#pragma unroll
      for (int tt = 0; tt < 4; ++tt) accb[tt] = smfma(oa1, ww[4 + tt], accb[tt], sidx);
    }
    {
      float sm[4];
#pragma unroll
      for (int tt = 0; tt < 4; ++tt) { sm[tt] = accb[tt].x + accb[tt].y; float t2 = accb[tt].z + accb[tt].w; asm volatile("" : "+v"(sm[tt]), "+v"(t2)); sm[tt] += t2; }
      auto s02 = __builtin_amdgcn_permlane32_swap(__float_as_uint(sm[0]), __float_as_uint(sm[2]), false, false);
      auto s13 = __builtin_amdgcn_permlane32_swap(__float_as_uint(sm[1]), __float_as_uint(sm[3]), false, false);
      auto sw = __builtin_amdgcn_permlane16_swap(__float_as_uint(__uint_as_float(s02[0]) + __uint_as_float(s02[1])), __float_as_uint(__uint_as_float(s13[0]) + __uint_as_float(s13[1])), false, false);
      __builtin_amdgcn_raw_buffer_store_b128(tag4(__uint_as_float(sw[0]) + __uint_as_float(sw[1]), tb), rws, fo, 0, AUX_SC1);
    }
    if (w == 0) {
      v4f y4 = {0.f, 0.f, 0.f, 0.f};
      int o0 = io, o1 = io;
      __builtin_amdgcn_s_sleep(PD);
#pragma unroll 1
      for (unsigned spins = 0; spins < SPIN_CAP; ++spins) {
        asm volatile("" : "+v"(o0), "+v"(o1)::"memory");
        const v4u g0 = __builtin_amdgcn_raw_buffer_load_b128(rws, o0, 0, AUX_SC1), g1 = __builtin_amdgcn_raw_buffer_load_b128(rws, o1 + 1024, 0, AUX_SC1);
        const bool got0 = o0 < OOB && has_tag(g0, tag), got1 = o1 < OOB && has_tag(g1, tag);
        y4 += got0 ? untag(g0) : v4f{0.f, 0.f, 0.f, 0.f};
        y4 += got1 ? untag(g1) : v4f{0.f, 0.f, 0.f, 0.f};
        o0 = got0 ? OOB : o0; o1 = got1 ? OOB : o1;
        if (__all(o0 >= OOB && o1 >= OOB)) break;
      }
#pragma unroll
      for (int e = 0; e < 4; ++e) { float t = y4[e]; t += dppf<XOR8>(t); t += dppf<ROR4>(t); y4[e] = rows_sum(t); }
      __builtin_amdgcn_raw_buffer_store_b64(v2u{pk_bf16(y4.x, y4.y), pk_bf16(y4.z, y4.w)}, rout, oo, 0, 0);
    }
    __builtin_amdgcn_endpgm();
  } else {
    Stream S;
    S.rk = mk_rsrc((void*)(ka[1] + kvbase), WIN * 256); S.rv = mk_rsrc((void*)(ka[2] + kvbase), WIN * 256); S.rws = mk_rsrc((void*)ka[6], WS_BYTES);
    S.voff = lane * 16 + w * 1024; S.so = R0 * 8192; S.lane = lane; S.L = L; S.w = w; S.tpo = ((j & 3) * 64 + b * 8 + xcd) * 16;
    S.acc = v4f{0.f, 0.f, 0.f, 0.f};
#pragma unroll
    for (int d = 0; d < 4; ++d) S.msk[d] = (c >> 1) == d ? ((c & 1) ? 0xffff0000u : 0x0000ffffu) : 0u;
    stream_step<R0>(S);
    LAS unsigned* op = (LAS unsigned*)(L + L_OP + w * OPP) + 8 * (c >> 1) + ((2 * c + r) & 3);
    asm volatile("" : "+v"(op));
    eat_vs<v_done(63), 32>(S);
    if (lane < 32) {
      op[0] = pk_bf16(S.acc.x * S.fac, S.acc.y * S.fac);
      op[4] = pk_bf16(S.acc.z * S.fac, S.acc.w * S.fac);
    }
    barrier();
    asm volatile("s_endpgm\n\t" STUB(2));
    __builtin_unreachable();
  }
}

}

extern "C" void kernel_launch(void* const* d_in, const int* in_sizes, int n_in, void* d_out, int out_size, void* d_ws, size_t ws_size,
                              hipStream_t stream) {
  fused_kernel<<<256, 1024, 0, stream>>>(d_in[0], d_in[1], d_in[2], d_in[3], d_in[6], d_in[7], d_ws, d_in[4], d_in[8], d_in[9], d_out);
}
```
